```python
import torch

def _softmax(x, axis=-1):
    return torch.softmax(x, dim=axis)
def _gelu(x):
    return torch.nn.functional.gelu(x, approximate="tanh")
def _silu(x):
    return torch.nn.functional.silu(x)
def _sigmoid(x):
    return torch.sigmoid(x)
def _layernorm(x, axis=-1, eps=1e-5):
    m = x.mean(dim=axis, keepdim=True)
    v = x.var(dim=axis, keepdim=True, unbiased=False)
    return (x - m) / torch.sqrt(v + eps)
def _scatter_add(tgt, idx, src, axis=0):
    return tgt.clone().index_add_(axis, idx, src)
def _segment_sum(src, seg, n):
    out = torch.zeros((n,) + src.shape[1:], dtype=src.dtype)
    return out.index_add_(0, seg, src)
def _pad(x, pw):
    flat = tuple(v for pair in reversed(pw) for v in pair)
    return torch.nn.functional.pad(x, flat)

kernel_name = 'parallel_sum__ssm_conv_s5__glu_gated_geglu__prompt'

def setup_inputs():

    _w = (lambda a: (lambda t: t.to(torch.bfloat16) if t.is_floating_point() else t)(torch.as_tensor(a)))
    def _A_setup():
        B, S, D = 1, 2048, 512
        P = D
        Lambda_re = -0.5 * (lambda _x: torch.exp(torch.as_tensor(_x)))((lambda *s: torch.randn(*s, dtype=torch.bfloat16) * 0.2)(P))
        Lambda_im = (lambda *s: torch.randn(*s, dtype=torch.bfloat16) * 0.4)(P) * 1.0
        B_re = (lambda *s: torch.randn(*s, dtype=torch.bfloat16) * 0.2)(P, D) * (D ** -0.5)
        B_im = (lambda *s: torch.randn(*s, dtype=torch.bfloat16) * 0.2)(P, D) * (D ** -0.5)
        C_re = (lambda *s: torch.randn(*s, dtype=torch.bfloat16) * 0.2)(D, P) * (P ** -0.5)
        C_im = (lambda *s: torch.randn(*s, dtype=torch.bfloat16) * 0.2)(D, P) * (P ** -0.5)
        log_dt = (lambda *s: torch.randn(*s, dtype=torch.bfloat16) * 0.2)(P) * 0.3 - 2.0
        Dskip = (lambda *s: torch.randn(*s, dtype=torch.bfloat16) * 0.05)(D)
        return {
            "x": _w((lambda *s: torch.randn(*s, dtype=torch.bfloat16) * 0.05)(B, S, D)),
            "Lambda_re": _w(Lambda_re),
            "Lambda_im": _w(Lambda_im),
            "B_re": _w(B_re),
            "B_im": _w(B_im),
            "C_re": _w(C_re),
            "C_im": _w(C_im),
            "log_dt": _w(log_dt),
            "Dskip": _w(Dskip),
        }
    _A_in = _A_setup()

    def _B_setup():
        B, S, Dm, Df = 1, 2048, 512, 1376
        return {
            "x": _w((lambda *s: torch.randn(*s, dtype=torch.bfloat16) * 0.05)(B, S, Dm)),
            "w_gate": _w((lambda *s: torch.randn(*s, dtype=torch.bfloat16) * 0.4)(Dm, Df) * (Dm ** -0.5)),
            "w_up": _w((lambda *s: torch.randn(*s, dtype=torch.bfloat16) * 0.4)(Dm, Df) * (Dm ** -0.5)),
            "w_down": _w((lambda *s: torch.randn(*s, dtype=torch.bfloat16) * 0.4)(Df, Dm) * (Df ** -0.5)),
        }
    _B_in = _B_setup()

    return {
        "A_x": _A_in["x"],
        "A_Lambda_re": _A_in["Lambda_re"],
        "A_Lambda_im": _A_in["Lambda_im"],
        "A_B_re": _A_in["B_re"],
        "A_B_im": _A_in["B_im"],
        "A_C_re": _A_in["C_re"],
        "A_C_im": _A_in["C_im"],
        "A_log_dt": _A_in["log_dt"],
        "A_Dskip": _A_in["Dskip"],
        "B_x": _B_in["x"],
        "B_w_gate": _B_in["w_gate"],
        "B_w_up": _B_in["w_up"],
        "B_w_down": _B_in["w_down"],
    }

def reference(A_x, A_Lambda_re, A_Lambda_im, A_B_re, A_B_im, A_C_re, A_C_im, A_log_dt, A_Dskip, B_x, B_w_gate, B_w_up, B_w_down):
    def _A_ref(x, Lambda_re, Lambda_im, B_re, B_im, C_re, C_im, log_dt, Dskip):
        Bsz, L, H = x.shape
        Lam = Lambda_re + 1j * Lambda_im
        Bmat = B_re + 1j * B_im
        Cmat = C_re + 1j * C_im
        dt = torch.exp(log_dt)
        Lam_bar = torch.exp(dt * Lam)
        B_bar = ((Lam_bar - 1.0) / Lam)[:, None] * Bmat
        Bu = torch.einsum("bld,pd->blp", x + 0j, B_bar)
        h = torch.zeros_like(Bu[:, 0, :])
        hs = []
        for t in range(L):
            h = Lam_bar[None, :] * h + Bu[:, t, :]
            hs.append(h)
        hseq = torch.stack(hs, 1)
        y = 2.0 * torch.einsum("blp,dp->bld", hseq, Cmat).real + x * Dskip[None, None, :]
        return y

    def _B_ref(x, w_gate, w_up, w_down):
        g = torch.einsum("bsd,df->bsf", x, w_gate)
        u = torch.einsum("bsd,df->bsf", x, w_up)
        h = _gelu(g) * u
        return torch.einsum("bsf,fd->bsd", h, w_down)

    x = A_x
    xn = _layernorm(x, axis=-1)
    return x + _A_ref(xn, A_Lambda_re, A_Lambda_im, A_B_re, A_B_im, A_C_re, A_C_im, A_log_dt, A_Dskip) + _B_ref(xn, B_w_gate, B_w_up, B_w_down)
```

```cpp
#include <hip/hip_runtime.h>
#include <hip/hip_bf16.h>
typedef unsigned short u16;
__device__ __forceinline__ float bf2f(u16 v){ return __uint_as_float(((unsigned)v)<<16); }
constexpr int L=2048, D=512, P=512, F=1376;
__global__ void k_ln(const u16* x, float* xn){
  int t=blockIdx.x; __shared__ float red[256];
  float a=bf2f(x[t*D+threadIdx.x]), b=bf2f(x[t*D+256+threadIdx.x]);
  red[threadIdx.x]=a+b; __syncthreads();
  for(int s=128;s>0;s>>=1){ if(threadIdx.x<s) red[threadIdx.x]+=red[threadIdx.x+s]; __syncthreads(); }
  float m=red[0]/D; __syncthreads();
  red[threadIdx.x]=(a-m)*(a-m)+(b-m)*(b-m); __syncthreads();
  for(int s=128;s>0;s>>=1){ if(threadIdx.x<s) red[threadIdx.x]+=red[threadIdx.x+s]; __syncthreads(); }
  float r=rsqrtf(red[0]/D+1e-5f);
  xn[t*D+threadIdx.x]=(a-m)*r; xn[t*D+256+threadIdx.x]=(b-m)*r;
}
__global__ void k_g1(const float* xn, const u16* Bre, const u16* Bim, const u16* wg, const u16* wu, float* bur, float* bui, float* hg){
  int t=blockIdx.x; __shared__ float xs[D];
  for(int i=threadIdx.x;i<D;i+=256) xs[i]=xn[t*D+i];
  __syncthreads();
  for(int p=threadIdx.x;p<P;p+=256){ float a=0,b=0; for(int k=0;k<D;k++){ a+=xs[k]*bf2f(Bre[p*D+k]); b+=xs[k]*bf2f(Bim[p*D+k]); } bur[t*P+p]=a; bui[t*P+p]=b; }
  for(int f=threadIdx.x;f<F;f+=256){ float g=0,u=0; for(int k=0;k<D;k++){ g+=xs[k]*bf2f(wg[k*F+f]); u+=xs[k]*bf2f(wu[k*F+f]); }
    float z=0.7978845608028654f*(g+0.044715f*g*g*g); hg[t*F+f]=0.5f*g*(1.f+tanhf(z))*u; }
}
__global__ void k_scan(const u16* lre,const u16* lim,const u16* ldt,float* bur,float* bui){
  int p=blockIdx.x*64+threadIdx.x;
  float dt=expf(bf2f(ldt[p])); dt=__bfloat162float(__float2bfloat16(dt));
  float lr=bf2f(lre[p]), li=bf2f(lim[p]);
  float e=expf(dt*lr), ar=e*cosf(dt*li), ai=e*sinf(dt*li);
  float nr=ar-1.f, ni=ai, den=lr*lr+li*li;
  float cr=(nr*lr+ni*li)/den, ci=(ni*lr-nr*li)/den;
  float hr=0,hi=0;
  for(int t=0;t<L;t++){ float br=bur[t*P+p], bi=bui[t*P+p]; float ur=cr*br-ci*bi, ui=cr*bi+ci*br;
    float nr2=ar*hr-ai*hi+ur, ni2=ar*hi+ai*hr+ui; hr=nr2; hi=ni2; bur[t*P+p]=hr; bui[t*P+p]=hi; }
}
__global__ void k_g2(const u16* x,const float* xn,const float* hr,const float* hi,const float* hg,const u16* Cre,const u16* Cim,const u16* wd,const u16* Ds,float* out){
  int t=blockIdx.x; __shared__ float s[2*P+F];
  for(int i=threadIdx.x;i<P;i+=256){ s[i]=hr[t*P+i]; s[P+i]=hi[t*P+i]; }
  for(int i=threadIdx.x;i<F;i+=256) s[2*P+i]=hg[t*F+i];
  __syncthreads();
  for(int d=threadIdx.x;d<D;d+=256){ float a=0; for(int p=0;p<P;p++) a+=s[p]*bf2f(Cre[d*P+p])-s[P+p]*bf2f(Cim[d*P+p]);
    float b=0; for(int f=0;f<F;f++) b+=s[2*P+f]*bf2f(wd[f*D+d]);
    out[t*D+d]=bf2f(x[t*D+d])+2.f*a+xn[t*D+d]*bf2f(Ds[d])+b; }
}
extern "C" void kernel_launch(void* const* d_in, const int* in_sizes, int n_in, void* d_out, int out_size, void* d_ws, size_t ws_size, hipStream_t stream) {
  const u16* x=(const u16*)d_in[0];
  float* ws=(float*)d_ws; float* xn=ws; float* bur=xn+L*D; float* bui=bur+L*P; float* hg=bui+L*P;
  k_ln<<<L,256,0,stream>>>(x,xn);
  k_g1<<<L,256,0,stream>>>(xn,(const u16*)d_in[3],(const u16*)d_in[4],(const u16*)d_in[10],(const u16*)d_in[11],bur,bui,hg);
  k_scan<<<P/64,64,0,stream>>>((const u16*)d_in[1],(const u16*)d_in[2],(const u16*)d_in[7],bur,bui);
  k_g2<<<L,256,0,stream>>>(x,xn,bur,bui,hg,(const u16*)d_in[5],(const u16*)d_in[6],(const u16*)d_in[12],(const u16*)d_in[8],(float*)d_out);
}
```

```cpp
#include <hip/hip_runtime.h>
#include <stdint.h>
#include <type_traits>

typedef unsigned short u16;
typedef __attribute__((ext_vector_type(4))) short s16x4;
typedef __attribute__((ext_vector_type(8))) short s16x8;
typedef __attribute__((ext_vector_type(8))) __bf16 bf16x8;
typedef __attribute__((ext_vector_type(2))) __bf16 bf16x2;
typedef __attribute__((ext_vector_type(4))) unsigned u32x4;
typedef __attribute__((ext_vector_type(2))) unsigned u32x2;
typedef __attribute__((ext_vector_type(4))) int i32x4;
typedef __attribute__((ext_vector_type(2))) float f32x2;
typedef __attribute__((ext_vector_type(4))) float f32x4;
typedef __attribute__((ext_vector_type(16))) float f32x16;
#define LDSP __attribute__((address_space(3)))
#define DI __device__ __forceinline__

constexpr int SEQ = 2048, DM = 512, FF = 1376;
constexpr unsigned NQ = 75;
constexpr unsigned WS_H = 0;
constexpr unsigned WS_AGG = 64u * NQ * 2048u;
constexpr unsigned WS_CTL = WS_AGG + 16u * 16u * 512u;
constexpr unsigned WS_EROW = WS_CTL + 4096u;
constexpr unsigned WS_ECOL = WS_CTL + 8192u;
constexpr unsigned WS_END = WS_CTL + 12288u;
constexpr unsigned OOB = 0x7ffffff0u;

constexpr unsigned SCR = 9u * 16384u;
constexpr unsigned L_CAR = SCR;
constexpr unsigned L_STP = SCR;
constexpr unsigned L_SEG = SCR + 8192u;
constexpr unsigned L_CSP = SCR + 12288u;
constexpr unsigned L_STF = SCR + 14336u;
constexpr unsigned L_CSF = SCR + 15360u;
constexpr unsigned LDS_BYTES = 163840u;
constexpr unsigned RB(int s) { return (unsigned)(s % 9) * 16384u; }

#define SPIN_CAP 20000

DI i32x4 mk_rsrc(const void* p, unsigned bytes) {
  unsigned long long a = (unsigned long long)p;
  i32x4 r;
  r[0] = (int)(unsigned)a; r[1] = (int)((unsigned)(a >> 32) & 0xffffu); r[2] = (int)bytes; r[3] = 0x00020000;
  return r;
}
DI void glds(i32x4 rs, unsigned voff, unsigned soff, unsigned lds) {
  asm volatile("s_mov_b32 m0, %3\n\ts_nop 0\n\tbuffer_load_dwordx4 %0, %1, %2 offen lds"
               :: "v"(voff), "s"(rs), "s"(soff), "s"(lds) : "memory");
}
DI void ld4_sc1(u32x4& r, i32x4 rs, unsigned voff, unsigned soff) {
  asm volatile("buffer_load_dwordx4 %0, %1, %2, %3 offen sc1" : "=v"(r) : "v"(voff), "s"(rs), "s"(soff) : "memory");
}
DI void ld4_sc1_1k(u32x4& r, i32x4 rs, unsigned voff, unsigned soff) {
  asm volatile("buffer_load_dwordx4 %0, %1, %2, %3 offen offset:1024 sc1" : "=v"(r) : "v"(voff), "s"(rs), "s"(soff) : "memory");
}
DI void ld1_sc1(unsigned& r, i32x4 rs, unsigned voff, unsigned soff) {
  asm volatile("buffer_load_dword %0, %1, %2, %3 offen sc1" : "=v"(r) : "v"(voff), "s"(rs), "s"(soff) : "memory");
}
DI void ld2(u32x2& r, i32x4 rs, unsigned voff, unsigned soff) {
  asm volatile("buffer_load_dwordx2 %0, %1, %2, %3 offen" : "=v"(r) : "v"(voff), "s"(rs), "s"(soff) : "memory");
}
DI void ldh(unsigned& r, i32x4 rs, unsigned voff) {
  asm volatile("buffer_load_ushort %0, %1, %2, 0 offen" : "=v"(r) : "v"(voff), "s"(rs) : "memory");
}
DI void st4_sc1(u32x4 v, i32x4 rs, unsigned voff, unsigned soff) {
  asm volatile("buffer_store_dwordx4 %0, %1, %2, %3 offen sc1\n\ts_nop 1" :: "v"(v), "v"(voff), "s"(rs), "s"(soff) : "memory");
}
DI void st1_sc1(unsigned v, i32x4 rs, unsigned voff, unsigned soff) {
  asm volatile("buffer_store_dword %0, %1, %2, %3 offen sc1" :: "v"(v), "v"(voff), "s"(rs), "s"(soff) : "memory");
}
template <int N> DI void wait_vm() { asm volatile("s_waitcnt vmcnt(%0)" :: "i"(N) : "memory"); }
DI void sched_fence() { __builtin_amdgcn_sched_barrier(0); }
template <int N> DI void wait_bar() {
  sched_fence();
  asm volatile("s_waitcnt vmcnt(%0) lgkmcnt(0)\n\ts_barrier" :: "i"(N) : "memory");
  sched_fence();
}
DI u32x4 lds128(unsigned a) { return *(LDSP u32x4*)a; }
DI f32x4 lds128f(unsigned a) { return *(LDSP f32x4*)a; }
DI f32x2 lds64f(unsigned a) { return *(LDSP f32x2*)a; }
DI u32x4 ldstr(unsigned a0, unsigned a1) {
  s16x4 lo = __builtin_amdgcn_ds_read_tr16_b64_v4i16((LDSP s16x4*)a0);
  s16x4 hi = __builtin_amdgcn_ds_read_tr16_b64_v4i16((LDSP s16x4*)a1);
  return __builtin_bit_cast(u32x4, __builtin_shufflevector(lo, hi, 0, 1, 2, 3, 4, 5, 6, 7));
}
DI f32x16 mfma(u32x4 a, u32x4 b, f32x16 c) {
  return __builtin_amdgcn_mfma_f32_32x32x16_bf16(__builtin_bit_cast(bf16x8, a), __builtin_bit_cast(bf16x8, b), c, 0, 0, 0);
}
DI float dot2(unsigned a, unsigned b, float c) {
  return __builtin_amdgcn_fdot2_f32_bf16(__builtin_bit_cast(bf16x2, a), __builtin_bit_cast(bf16x2, b), c, false);
}
DI unsigned pk(float lo, float hi) {
  f32x2 p = {lo, hi};
  return __builtin_bit_cast(unsigned, __builtin_convertvector(p, bf16x2));
}
DI float bf2f(unsigned v) { return __uint_as_float(v << 16); }
DI float halfsum(float a, float b) {
  auto r = __builtin_amdgcn_permlane32_swap(__float_as_uint(a), __float_as_uint(b), false, false);
  return __uint_as_float(r[0]) + __uint_as_float(r[1]);
}
DI unsigned tau(unsigned r) { return 16u * ((r >> 2) & 1u) + (r & 3u) + 4u * (r >> 3); }
DI unsigned swap23(unsigned r) { return (r & 0x13u) | ((r & 4u) << 1) | ((r & 8u) >> 1); }
constexpr unsigned ONES = 0x3F803F80u;

#define STAMP(i) do {} while (0)

struct Ctx {
  i32x4 rsX, rsWA, rsWG, rsWD, rsC, rsWS;
  unsigned vX[2], vWA[2], vWG[2], vWD[2], vC[2];
  unsigned sX, sWA, sWAstep, sWB, sWD, sC;
  unsigned ldsw;
  unsigned rotc;
};

template <int S> DI void fill(const Ctx& c) {
  const unsigned l = RB(S) + c.ldsw;
  if constexpr (S < 16) {
    const unsigned ch = (unsigned)((S >> 1) + c.rotc) & 7u;
    if constexpr ((S & 1) == 0) { const unsigned so = c.sX + ch * 128u; glds(c.rsX, c.vX[0], so, l); glds(c.rsX, c.vX[1], so, l + 1024u); }
    else { const unsigned so = c.sWA + ch * c.sWAstep; glds(c.rsWA, c.vWA[0], so, l); glds(c.rsWA, c.vWA[1], so, l + 1024u); }
  } else if constexpr (S < 24) {
    const unsigned ch = (unsigned)((S - 16) + c.rotc) & 7u;
    const unsigned so = c.sWB + ch * (64u * FF * 2u);
    glds(c.rsWG, c.vWG[0], so, l); glds(c.rsWG, c.vWG[1], so, l + 1024u);
  } else if constexpr (S < 43) {
    constexpr int i = S - 24;
    if constexpr (i >= 4 && i < 12) { const unsigned so = c.sC + (unsigned)(i - 4) * 128u; glds(c.rsC, c.vC[0], so, l); glds(c.rsC, c.vC[1], so, l + 1024u); }
    else { const unsigned so = c.sWD + (unsigned)(i < 4 ? i : i - 8) * (128u * DM * 2u); glds(c.rsWD, c.vWD[0], so, l); glds(c.rsWD, c.vWD[1], so, l + 1024u); }
  }
}

template <int I, int N, class Fn> DI void static_for(Fn&& f) {
  if constexpr (I < N) { f(std::integral_constant<int, I>{}); static_for<I + 1, N>(f); }
}

DI void poll_flags(unsigned v, bool sel, unsigned tag, i32x4 rs, unsigned voff, unsigned soff) {
  if (__all(!sel || v == tag)) return;
  for (int it = 0; it < SPIN_CAP; ++it) {
    __builtin_amdgcn_s_sleep(1);
    unsigned w;
    asm volatile("buffer_load_dword %0, %1, %2, %3 offen sc1\n\ts_waitcnt vmcnt(0)" : "=&v"(w) : "v"(voff), "s"(rs), "s"(soff) : "memory");
    if (__all(!sel || w == tag)) return;
  }
}

DI void glu_epilogue(const f32x16& a0, const f32x16& a1, float ra, float rb, unsigned cg, unsigned r, unsigned h, u32x4& o0, u32x4& o1) {
  const f32x4 c4 = lds128f(L_CSP + ((cg * 2u + h) * 32u + r) * 16u);
  *(LDSP float*)(L_CSF + ((cg * 2u + h) * 32u + r) * 4u) = (c4[0] + c4[1]) + (c4[2] + c4[3]);
  asm volatile("" ::: "memory");
  float hv[16];
  constexpr float K1 = -2.302208198f;
  constexpr float K2 = -0.1029432397f;
#pragma unroll
  for (int g = 0; g < 4; ++g) {
    const f32x4 cg4 = lds128f(L_CSF + cg * 256u + (8u * g + 4u * h) * 4u);
    const f32x4 cu4 = lds128f(L_CSF + cg * 256u + 128u + (8u * g + 4u * h) * 4u);
#pragma unroll
    for (int e = 0; e < 4; ++e) {
      const float gv = ra * a0[4 * g + e] + rb * cg4[e];
      const float uv = ra * a1[4 * g + e] + rb * cu4[e];
      const float z = gv * (K1 + K2 * gv * gv);
      const float s = __builtin_amdgcn_rcpf(1.0f + __builtin_amdgcn_exp2f(z));
      hv[4 * g + e] = gv * s * uv;
    }
  }
#pragma unroll
  for (int e = 0; e < 4; ++e) { o0[e] = pk(hv[2 * e], hv[2 * e + 1]); o1[e] = pk(hv[8 + 2 * e], hv[9 + 2 * e]); }
}

constexpr int G1 = 4, G2 = 5;
constexpr int DA = 6;
constexpr int ops_at(int i) {
  return ((i >= 1 && i <= 10) ? 2 : 0) + ((i + DA <= 18) ? 2 : 0) + (i == 2 ? 1 : 0) + (i == 4 ? 1 : 0) + (i == 13 ? 3 : 0);
}
constexpr int ops_sum(int a, int b) { int s = 0; for (int i = a; i <= b; ++i) s += ops_at(i); return s; }
constexpr int wait_at(int i) {
  int n = 63;
  if (i == 4) n = 2 + ops_sum(0, 3);
  if (i == 5) n = ops_sum(0, 4);
  if (i >= 6) n = ops_sum(i - 5, i - 1);
  if (i == 2) n = 4 + ops_sum(0, 1);
  if (i == 6) { int m = 4 + ops_sum(5, 5); n = m < n ? m : n; }
  return n > 63 ? 63 : n;
}

template <bool SSM>
DI void phase1(const Ctx& c, unsigned rt, unsigned s, unsigned wv, unsigned lane, unsigned tagR, unsigned tagC,
               float lr, float li, float cr, float ci, f32x16& acc0, f32x16& acc1, float& ra, float& rb, unsigned& poll1,
               unsigned* stamp) {
  const unsigned rg = wv & 3u, cg = wv >> 2, r = lane & 31u, h = lane >> 5, j = s >> 1;
  const unsigned rot = rg ^ (2u * cg);
  unsigned xa[4], ga[4];
#pragma unroll
  for (int i = 0; i < 4; ++i) {
    xa[i] = 128u * r + 16u * ((2u * (i ^ rot) + h) ^ ((r >> 1) & 7u));
    const unsigned q = (lane & 15u) >> 2;
    ga[i] = 128u * (16u * (i ^ rot) + 8u * h + q) + 64u * (cg ^ (q >> 1)) + 32u * ((lane >> 4) & 1u) + 8u * (lane & 3u);
  }
  u32x4 xf[32];
  float sx = 0.f, sq = 0.f, c0 = 0.f, c1 = 0.f;
  acc0 = f32x16{}; acc1 = f32x16{};
  static_for<0, 8>([&](auto CC) {
    constexpr int C = CC;
    wait_bar<C == 0 ? 14 : 10>();
    if constexpr (C >= 1) { fill<2 * C + 7>(c); fill<2 * C + 8>(c); }
    if constexpr (C == 0) STAMP(1);
#pragma unroll
    for (int i = 0; i < 4; ++i) {
      const u32x4 x = lds128(xa[i] + rg * 4096u + RB(2 * C));
      xf[4 * C + i] = x;
      u32x4 w0, w1;
      if constexpr (SSM) {
        w0 = lds128(xa[i] + cg * 8192u + RB(2 * C + 1)); w1 = lds128(xa[i] + cg * 8192u + 4096u + RB(2 * C + 1));
        acc0 = mfma(x, w0, acc0); acc1 = mfma(x, w1, acc1);
      } else {
        w0 = ldstr(ga[i] + RB(2 * C + 1), ga[i] + RB(2 * C + 1) + 512u);
        w1 = ldstr(ga[i] + RB(2 * C + 1) + 8192u, ga[i] + RB(2 * C + 1) + 8704u);
        acc0 = mfma(w0, x, acc0); acc1 = mfma(w1, x, acc1);
      }
      if (i == 0) {
#pragma unroll
        for (int e = 0; e < 4; ++e) { c0 = dot2(w0[e], ONES, c0); c1 = dot2(w1[e], ONES, c1); }
      }
      if (i < 2) {
#pragma unroll
        for (int e = 0; e < 4; ++e) { sx = dot2(x[e], ONES, sx); sq = dot2(x[e], x[e], sq); }
      }
    }
  });
  STAMP(2);
  *(LDSP float*)(L_STP + (rg * 32u + r) * 16u + (cg * 2u + h) * 4u) = halfsum(sx, sq);
  *(LDSP float*)(L_CSP + ((cg * 2u + h) * 32u + r) * 16u + rg * 4u) = halfsum(c0, c1);
  wait_bar<63>();
  fill<23>(c); fill<24>(c);
  {
    const f32x4 st = lds128f(L_STP + (rg * 32u + r) * 16u);
    const float mean = (st[0] + st[2]) * (1.0f / DM);
    const float var = fmaxf((st[1] + st[3]) * (1.0f / DM) - mean * mean, 0.0f);
    ra = __builtin_amdgcn_rsqf(var + 1e-5f);
    rb = -mean * ra;
    *(LDSP f32x2*)(L_STF + (rg * 32u + tau(r)) * 8u) = f32x2{ra, rb};
    asm volatile("" ::: "memory");
  }
  const unsigned hrow = (rt * 4u + rg) * NQ;
  f32x16 ur, ui;
  float l16r = lr, l16i = li;
  if constexpr (SSM) {
    const f32x4 a4 = lds128f(L_CSP + ((cg * 2u) * 32u + r) * 16u), b4 = lds128f(L_CSP + ((cg * 2u + 1u) * 32u + r) * 16u);
    const float csr = (a4[0] + a4[1]) + (a4[2] + a4[3]), csi = (b4[0] + b4[1]) + (b4[2] + b4[3]);
    float er = 0.f, ei = 0.f;
#pragma unroll
    for (int t = 0; t < 8; ++t) {
      const f32x4 ab = lds128f(L_STF + (rg * 32u + 16u * h + 2u * t) * 8u);
#pragma unroll
      for (int e = 0; e < 2; ++e) {
        const float vr = ab[2 * e] * acc0[2 * t + e] + ab[2 * e + 1] * csr;
        const float vi = ab[2 * e] * acc1[2 * t + e] + ab[2 * e + 1] * csi;
        ur[2 * t + e] = vr; ui[2 * t + e] = vi;
        const float nr = lr * er + (vr - li * ei), ni = lr * ei + (vi + li * er);
        er = nr; ei = ni;
      }
    }
    *(LDSP f32x2*)(L_SEG + ((cg * 8u + 2u * rg + h) * 32u + r) * 8u) = f32x2{er, ei};
#pragma unroll
    for (int t = 0; t < 4; ++t) { const float a = l16r * l16r - l16i * l16i, b = 2.0f * l16r * l16i; l16r = a; l16i = b; }
  } else {
    u32x4 o0, o1;
    glu_epilogue(acc0, acc1, ra, rb, cg, r, h, o0, o1);
    const unsigned so = WS_H + (hrow + 2u * j + cg) * 2048u;
    st4_sc1(o0, c.rsWS, lane * 16u, so); st4_sc1(o1, c.rsWS, lane * 16u + 1024u, so);
  }
  STAMP(3);
  acc0 = f32x16{}; acc1 = f32x16{};
  c0 = 0.f; c1 = 0.f;
  u32x4 g0, g1;
  const unsigned j1 = 2u * rg + h, j2 = j1 + 8u;
  const unsigned gv1 = j1 < rt ? (j1 * 32u + r) * 16u : OOB, gv2 = j2 < rt ? (j2 * 32u + r) * 16u : OOB;
  const unsigned gso = WS_AGG + (2u * j + cg) * 8192u;
  static_for<0, 8>([&](auto CC) {
    constexpr int C = CC;
    if constexpr (SSM && C == G2) {
      asm volatile("s_waitcnt vmcnt(%2)" : "+v"(g0), "+v"(g1) : "i"(2 * (G2 - G1 - 1)) : "memory");
      bool ok = (j1 >= rt || (g0[1] == tagC && g0[3] == tagC)) && (j2 >= rt || (g1[1] == tagC && g1[3] == tagC));
      if (!__all(ok)) {
        for (int it = 0; it < SPIN_CAP; ++it) {
          __builtin_amdgcn_s_sleep(1);
          asm volatile("buffer_load_dwordx4 %0, %2, %4, %5 offen sc1\n\tbuffer_load_dwordx4 %1, %3, %4, %5 offen sc1\n\ts_waitcnt vmcnt(0)"
                       : "=&v"(g0), "=&v"(g1) : "v"(gv1), "v"(gv2), "s"(c.rsWS), "s"(gso) : "memory");
          ok = (j1 >= rt || (g0[1] == tagC && g0[3] == tagC)) && (j2 >= rt || (g1[1] == tagC && g1[3] == tagC));
          if (__all(ok)) break;
        }
      }
      *(LDSP u32x2*)(L_CAR + ((cg * 16u + j1) * 32u + r) * 8u) = u32x2{g0[0], g0[2]};
      *(LDSP u32x2*)(L_CAR + ((cg * 16u + j2) * 32u + r) * 8u) = u32x2{g1[0], g1[2]};
    }
    constexpr int NW = (!SSM && C == 4) ? 6 : (C == 0 ? 16 : 14);
    wait_bar<NW>();
    if constexpr (C >= 1) fill<24 + C>(c);
    if constexpr (!SSM && C == 4) st1_sc1(tagR, c.rsWS, (wv == 0 && lane == 0) ? s * 4u : OOB, WS_CTL + rt * 256u);
    if constexpr (SSM && C == 0) {
      float er = 0.f, ei = 0.f;
#pragma unroll
      for (int t = 0; t < 8; ++t) {
        const f32x2 e = lds64f(L_SEG + ((cg * 8u + t) * 32u + r) * 8u);
        const float nr = l16r * er + (e[0] - l16i * ei), ni = l16r * ei + (e[1] + l16i * er);
        er = nr; ei = ni;
      }
      st4_sc1(u32x4{__float_as_uint(er), tagC, __float_as_uint(ei), tagC}, c.rsWS, (rg == 0 && h == 0) ? (rt * 32u + r) * 16u : OOB, gso);
    }
    if constexpr (SSM && C == G1) { ld4_sc1(g0, c.rsWS, gv1, gso); ld4_sc1(g1, c.rsWS, gv2, gso); }
    if constexpr (C == 6) ld1_sc1(poll1, c.rsWS, (lane & 15u) * 4u, WS_CTL + rt * 256u);
    if constexpr (SSM && C == G2) {
      STAMP(4);
      st1_sc1(tagC, c.rsWS, (rt == 15u && wv == 0 && lane == 0) ? j * 128u : OOB, WS_ECOL);
      float pr = l16r, pi = l16i;
#pragma unroll
      for (int t = 0; t < 3; ++t) { const float a = pr * pr - pi * pi, b = 2.0f * pr * pi; pr = a; pi = b; }
      float er = 0.f, ei = 0.f;
      for (unsigned t = 0; t < rt; ++t) {
        const f32x2 e = lds64f(L_CAR + ((cg * 16u + t) * 32u + r) * 8u);
        const float nr = pr * er + (e[0] - pi * ei), ni = pr * ei + (e[1] + pi * er);
        er = nr; ei = ni;
      }
#pragma unroll
      for (int t = 0; t < 7; ++t) {
        const f32x2 e = lds64f(L_SEG + ((cg * 8u + t) * 32u + r) * 8u);
        const float nr = l16r * er + (e[0] - l16i * ei), ni = l16r * ei + (e[1] + l16i * er);
        const bool use = (unsigned)t < j1;
        er = use ? nr : er; ei = use ? ni : ei;
      }
      unsigned pr_[8], pi_[8];
#pragma unroll
      for (int t = 0; t < 8; ++t) {
        float yr[2], yi[2];
#pragma unroll
        for (int e = 0; e < 2; ++e) {
          const float nr = lr * er + (ur[2 * t + e] - li * ei), ni = lr * ei + (ui[2 * t + e] + li * er);
          er = nr; ei = ni;
          yr[e] = cr * er - ci * ei; yi[e] = -(cr * ei + ci * er);
        }
        pr_[t] = pk(yr[0], yr[1]); pi_[t] = pk(yi[0], yi[1]);
      }
      u32x4 id[2];
#pragma unroll
      for (int ks = 0; ks < 2; ++ks)
#pragma unroll
        for (int e = 0; e < 4; ++e) {
          const int jj = 8 * ks + 2 * e;
          const unsigned r0 = (jj & 3) + 8 * (jj >> 2) + 4u * h;
          id[ks][e] = r == r0 ? 0x3F80u : (r == r0 + 1u ? 0x3F800000u : 0u);
        }
      f32x16 zr = mfma(u32x4{pr_[0], pr_[1], pr_[2], pr_[3]}, id[0], f32x16{});
      f32x16 zi = mfma(u32x4{pi_[0], pi_[1], pi_[2], pi_[3]}, id[0], f32x16{});
      zr = mfma(u32x4{pr_[4], pr_[5], pr_[6], pr_[7]}, id[1], zr);
      zi = mfma(u32x4{pi_[4], pi_[5], pi_[6], pi_[7]}, id[1], zi);
      u32x4 o[4];
#pragma unroll
      for (int e = 0; e < 4; ++e) {
        o[0][e] = pk(zr[2 * e], zr[2 * e + 1]); o[1][e] = pk(zr[8 + 2 * e], zr[9 + 2 * e]);
        o[2][e] = pk(zi[2 * e], zi[2 * e + 1]); o[3][e] = pk(zi[8 + 2 * e], zi[9 + 2 * e]);
      }
      const unsigned so = WS_H + (hrow + 16u + 4u * j + cg) * 2048u;
      st4_sc1(o[0], c.rsWS, lane * 16u, so); st4_sc1(o[1], c.rsWS, lane * 16u + 1024u, so);
      st4_sc1(o[2], c.rsWS, lane * 16u, so + 4096u); st4_sc1(o[3], c.rsWS, lane * 16u + 1024u, so + 4096u);
      STAMP(5);
      sched_fence();
    }
#pragma unroll
    for (int i = 0; i < 4; ++i) {
      const u32x4 w0 = ldstr(ga[i] + RB(16 + C), ga[i] + RB(16 + C) + 512u);
      const u32x4 w1 = ldstr(ga[i] + RB(16 + C) + 8192u, ga[i] + RB(16 + C) + 8704u);
      acc0 = mfma(w0, xf[4 * C + i], acc0); acc1 = mfma(w1, xf[4 * C + i], acc1);
      if (i == 0) {
#pragma unroll
        for (int e = 0; e < 4; ++e) { c0 = dot2(w0[e], ONES, c0); c1 = dot2(w1[e], ONES, c1); }
      }
    }
  });
  STAMP(6);
  *(LDSP float*)(L_CSP + ((cg * 2u + h) * 32u + r) * 16u + rg * 4u) = halfsum(c0, c1);
  wait_bar<SSM ? 2 * (7 - G2) + 1 : 63>();
  if constexpr (SSM) st1_sc1(tagR, c.rsWS, (wv == 0 && lane == 0) ? s * 4u : OOB, WS_CTL + rt * 256u);
  fill<32>(c);
  asm volatile("s_waitcnt vmcnt(4)" : "+v"(poll1) :: "memory");
}

__global__ __launch_bounds__(512, 2) void s5_geglu_kernel(
    const u16* __restrict__ x, const u16* __restrict__ lre, const u16* __restrict__ lim, const u16* __restrict__ Bre,
    const u16* __restrict__ Bim, const u16* __restrict__ Cre, const u16* __restrict__ Cim, const u16* __restrict__ ldt,
    const u16* __restrict__ Dsk, const u16* __restrict__ wg, const u16* __restrict__ wu, const u16* __restrict__ wd,
    float* __restrict__ out, char* __restrict__ ws) {
  __shared__ __attribute__((aligned(16))) char smem[LDS_BYTES];
  unsigned stamp[16];
  STAMP(0);
  asm volatile("" :: "v"((unsigned)(uintptr_t)(LDSP char*)smem));
  const unsigned b = blockIdx.x, xcd = b & 7u, idx = b >> 3;
  unsigned rt = (xcd >> 1) * 4u + (idx & 3u);
  const unsigned s = (xcd & 1u) * 8u + (idx >> 2);
  const unsigned lane = threadIdx.x & 63u;
  unsigned wv = __builtin_amdgcn_readfirstlane(threadIdx.x >> 6);
  const bool ssm = (s & 1u) == 0u;
  const unsigned j = s >> 1, r = lane & 31u, h = lane >> 5, cg = wv >> 2;

  Ctx c;
  c.rsX = mk_rsrc(x, SEQ * DM * 2);
  c.rsWG = mk_rsrc(wv < 4u ? wg : wu, DM * FF * 2);
  c.rsWA = ssm ? mk_rsrc((wv & 2u) ? Bim : Bre, DM * DM * 2) : c.rsWG;
  c.rsWD = mk_rsrc(wd, FF * DM * 2);
  c.rsC = mk_rsrc(wv < 4u ? Cre : Cim, DM * DM * 2);
  c.rsWS = mk_rsrc(ws, WS_END);
  c.ldsw = wv * 2048u;
  c.rotc = 0;
#pragma unroll
  for (unsigned t = 0; t < 2; ++t) {
    const unsigned row = 16u * wv + 8u * t + (lane >> 3), cp = lane & 7u;
    const unsigned sw = (cp ^ ((row >> 1) & 7u)) * 16u;
    const unsigned hs = (((cp >> 2) ^ ((row >> 1) & 1u)) * 4u + (cp & 3u)) * 16u;
    c.vX[t] = ((row & 96u) + tau(row & 31u)) * 1024u + sw;
    c.vWG[t] = (row & 63u) * (FF * 2u) + hs;
    c.vWA[t] = ssm ? ((row >> 6) * 32u + swap23(row & 31u)) * 1024u + sw : c.vWG[t];
    c.vWD[t] = row * 1024u + hs;
    c.vC[t] = (row & 63u) * 1024u + sw;
  }
  const unsigned pairB = ssm ? 16u + (j & 3u) + 3u * (j >> 2) : 8u + j;
  const bool hasB = !(ssm && (j & 3u) == 3u);
  c.sX = rt * (128u * 1024u);
  c.sWA = ssm ? j * 65536u : j * 128u;
  c.sWAstep = ssm ? 128u : 64u * FF * 2u;
  c.sWB = hasB ? pairB * 128u : 0x40000000u;
  c.sWD = j * 128u;
  c.sC = j * 65536u;

  unsigned eR, eC, plr, pli, pdt;
  {
    asm volatile("s_nop 4" ::: "memory");
    ld1_sc1(eR, c.rsWS, 0u, WS_EROW + rt * 128u);
    ld1_sc1(eC, c.rsWS, 0u, WS_ECOL + j * 128u);
    const unsigned po = (64u * j + 32u * cg + swap23(r)) * 2u;
    ldh(plr, mk_rsrc(lre, 1024), po); ldh(pli, mk_rsrc(lim, 1024), po); ldh(pdt, mk_rsrc(ldt, 1024), po);
  }
  static_for<0, 9>([&](auto S) { fill<S>(c); });
  asm volatile("s_waitcnt vmcnt(18)" : "+v"(eR), "+v"(eC), "+v"(plr), "+v"(pli), "+v"(pdt) :: "memory");
  const unsigned tagR = eR + 1u, tagC = eC + 1u;

  f32x16 acc0, acc1;
  float ra, rb;
  unsigned poll1;
  if (ssm) {
    const float dt = bf2f(pk(expf(bf2f(pdt)), 0.f) & 0xffffu);
    const float Lr = bf2f(plr), Li = bf2f(pli);
    const float e = expf(dt * Lr);
    float sn, cs;
    sincosf(dt * Li, &sn, &cs);
    const float lr = e * cs, li = e * sn;
    const float nr = lr - 1.0f, inv = 2.0f / (Lr * Lr + Li * Li);
    const float cr = (nr * Lr + li * Li) * inv, ci = (li * Lr - nr * Li) * inv;
    phase1<true>(c, rt, s, wv, lane, tagR, tagC, lr, li, cr, ci, acc0, acc1, ra, rb, poll1, stamp);
  } else {
    phase1<false>(c, rt, s, wv, lane, tagR, tagC, 0.f, 0.f, 0.f, 0.f, acc0, acc1, ra, rb, poll1, stamp);
  }

  asm volatile("" : "+s"(rt), "+s"(wv));
  const unsigned kg = wv & 3u, rh2 = wv >> 2, rh = s & 1u, rg = wv & 3u;
  const unsigned fso = WS_CTL + rt * 256u, fvo = (lane & 15u) * 4u;
  const unsigned aso = WS_H + ((rt * 4u + rh * 2u + rh2) * NQ + kg) * 2048u, avo = lane * 16u;
  u32x4 A[19][2];
  auto lda = [&](auto II) { constexpr int I = II; ld4_sc1(A[I][0], c.rsWS, avo, aso + I * 8192u); ld4_sc1_1k(A[I][1], c.rsWS, avo, aso + I * 8192u); };
  poll_flags(poll1, (lane & 1u) != 0u, tagR, c.rsWS, fvo, fso);
  static_for<0, 4>(lda);
  {
    u32x4 o0, o1;
    glu_epilogue(acc0, acc1, ra, rb, cg, r, h, o0, o1);
    unsigned poll2;
    ld1_sc1(poll2, c.rsWS, fvo, fso);
    const unsigned u = 2u * pairB + cg;
    const unsigned so = (hasB && u < 43u) ? WS_H + ((rt * 4u + rg) * NQ + u + 32u) * 2048u : 0x40000000u;
    st4_sc1(o0, c.rsWS, avo, so); st4_sc1(o1, c.rsWS, avo + 1024u, so);
    STAMP(7);
    asm volatile("s_waitcnt vmcnt(2)" : "+v"(poll2), "+v"(A[0][0]), "+v"(A[0][1]), "+v"(A[1][0]), "+v"(A[1][1]),
                 "+v"(A[2][0]), "+v"(A[2][1]), "+v"(A[3][0]), "+v"(A[3][1]) :: "memory");
    poll_flags(poll2, (lane & 1u) == 0u, tagR, c.rsWS, fvo, fso);
  }
  static_for<4, DA>(lda);
  STAMP(8);
  unsigned pa[2], ca[2];
  {
    const unsigned q = (lane & 15u) >> 2;
#pragma unroll
    for (unsigned t = 0; t < 2; ++t) {
      pa[t] = 128u * (32u * kg + 4u * h + q) + 64u * (t ^ (q >> 1)) + 32u * ((lane >> 4) & 1u) + 8u * (lane & 3u);
      ca[t] = (kg >> 1) * 8192u + 128u * r + 16u * ((4u * (kg & 1u) + 2u * t + h) ^ ((r >> 1) & 7u));
    }
  }
  acc0 = f32x16{}; acc1 = f32x16{};
  unsigned poll3;
  u32x2 xv[2], dv;
  const unsigned orow = rh * 64u + tau(threadIdx.x >> 4), ocol = j * 64u + (threadIdx.x & 15u) * 4u;
  static_for<0, 19>([&](auto II) {
    constexpr int I = II;
    sched_fence();
    if constexpr (I == 6) {
      asm volatile("s_waitcnt vmcnt(%3)" : "+v"(A[I][0]), "+v"(A[I][1]), "+v"(poll3) : "i"(wait_at(I)) : "memory");
      poll_flags(poll3, true, tagR, c.rsWS, fvo, fso + 128u);
      STAMP(9);
      wait_bar<63>();
      st1_sc1(tagR, c.rsWS, (s == 0u && wv == 0u && lane == 0u) ? rt * 128u : OOB, WS_EROW);
    } else {
      if constexpr (I >= 4) asm volatile("s_waitcnt vmcnt(%2)" : "+v"(A[I][0]), "+v"(A[I][1]) : "i"(wait_at(I)) : "memory");
      wait_bar<I == 2 ? wait_at(2) : 63>();
    }
    if constexpr (I == 2) st1_sc1(tagR, c.rsWS, (wv == 0u && lane == 0u) ? s * 4u : OOB, fso + 128u);
    if constexpr (I == 4) ld1_sc1(poll3, c.rsWS, fvo, fso + 128u);
    if constexpr (I >= 1 && I <= 10) fill<32 + I>(c);
    if constexpr (I + DA <= 18) lda(std::integral_constant<int, (I + DA <= 18 ? I + DA : 18)>{});
    if constexpr (I == 13) {
      ld2(xv[0], c.rsX, orow * 1024u + ocol * 2u, c.sX); ld2(xv[1], c.rsX, orow * 1024u + ocol * 2u, c.sX + 32768u);
      ld2(dv, mk_rsrc(Dsk, 1024), ocol * 2u, 0u);
    }
    constexpr unsigned B0 = RB(24 + I);
    if (I < 18 || kg != 3u) {
#pragma unroll
      for (int ks = 0; ks < 2; ++ks) {
        u32x4 w0, w1;
        if constexpr (I >= 4 && I < 12) { w0 = lds128(ca[ks] + B0); w1 = lds128(ca[ks] + B0 + 4096u); }
        else {
          w0 = ldstr(pa[0] + B0 + 2048u * ks, pa[0] + B0 + 2048u * ks + 1024u);
          w1 = ldstr(pa[1] + B0 + 2048u * ks, pa[1] + B0 + 2048u * ks + 1024u);
        }
        acc0 = mfma(w0, A[I][ks], acc0); acc1 = mfma(w1, A[I][ks], acc1);
      }
    }
  });
  STAMP(10);
  wait_bar<63>();
#pragma unroll
  for (int g = 0; g < 4; ++g) {
    const unsigned a = ((kg * 64u + rh2 * 32u + r) * 68u + 8u * g + 4u * h) * 4u;
    *(LDSP f32x4*)a = f32x4{acc0[4 * g], acc0[4 * g + 1], acc0[4 * g + 2], acc0[4 * g + 3]};
    *(LDSP f32x4*)(a + 128u) = f32x4{acc1[4 * g], acc1[4 * g + 1], acc1[4 * g + 2], acc1[4 * g + 3]};
  }
  sched_fence();
  asm volatile("s_waitcnt vmcnt(0) lgkmcnt(0)\n\ts_barrier" : "+v"(xv[0]), "+v"(xv[1]), "+v"(dv) :: "memory");
  sched_fence();
#pragma unroll
  for (unsigned m = 0; m < 2; ++m) {
    const unsigned a = ((m * 32u + (threadIdx.x >> 4)) * 68u + (threadIdx.x & 15u) * 4u) * 4u;
    const f32x4 p = (lds128f(a) + lds128f(a + 17408u)) + (lds128f(a + 34816u) + lds128f(a + 52224u));
    const f32x2 ab = lds64f(L_STF + (orow + 32u * m) * 8u);
    f32x4 o;
#pragma unroll
    for (int e = 0; e < 4; ++e) {
      const float xe = (e & 1) ? __uint_as_float(xv[m][e >> 1] & 0xffff0000u) : bf2f(xv[m][e >> 1] & 0xffffu);
      const float de = (e & 1) ? __uint_as_float(dv[e >> 1] & 0xffff0000u) : bf2f(dv[e >> 1] & 0xffffu);
      o[e] = p[e] + xe + (xe * ab[0] + ab[1]) * de;
    }
    *(f32x4*)(out + (size_t)(rt * 128u + orow + 32u * m) * DM + ocol) = o;
  }
}

extern "C" void kernel_launch(void* const* d_in, const int* in_sizes, int n_in, void* d_out, int out_size, void* d_ws,
                              size_t ws_size, hipStream_t stream) {
  s5_geglu_kernel<<<256, 512, 0, stream>>>(
      (const u16*)d_in[0], (const u16*)d_in[1], (const u16*)d_in[2], (const u16*)d_in[3], (const u16*)d_in[4],
      (const u16*)d_in[5], (const u16*)d_in[6], (const u16*)d_in[7], (const u16*)d_in[8], (const u16*)d_in[10],
      (const u16*)d_in[11], (const u16*)d_in[12], (float*)d_out, (char*)d_ws);
}
```

```cpp
#include <hip/hip_runtime.h>
#include <stdint.h>
#include <type_traits>

typedef unsigned short u16;
typedef __attribute__((ext_vector_type(4))) short s16x4;
typedef __attribute__((ext_vector_type(8))) __bf16 bf16x8;
typedef __attribute__((ext_vector_type(2))) __bf16 bf16x2;
typedef __attribute__((ext_vector_type(4))) unsigned u32x4;
typedef __attribute__((ext_vector_type(2))) unsigned u32x2;
typedef __attribute__((ext_vector_type(4))) int i32x4;
typedef __attribute__((ext_vector_type(2))) float f32x2;
typedef __attribute__((ext_vector_type(4))) float f32x4;
typedef __attribute__((ext_vector_type(16))) float f32x16;
#define LDSP __attribute__((address_space(3)))
#define DI __device__ __forceinline__
template <int V> using IC = std::integral_constant<int, V>;

constexpr unsigned SEQ = 2048, DM = 512, FF = 1376;
constexpr unsigned NQ = 75;
constexpr unsigned WS_H = 0;
constexpr unsigned WS_AGG = 64u * NQ * 2048u;
constexpr unsigned WS_CTL = WS_AGG + 16u * 16u * 512u;
constexpr unsigned WS_EROW = WS_CTL + 4096u;
constexpr unsigned WS_ECOL = WS_CTL + 8192u;
constexpr unsigned WS_END = WS_CTL + 12288u;
constexpr unsigned OOB = 0x7ffffff0u;
constexpr unsigned SOOB = 0x40000000u;

constexpr unsigned SCR = 9u * 16384u;
constexpr unsigned L_STP = SCR;
constexpr unsigned L_CAR = SCR + 2048u;
constexpr unsigned L_SEG = SCR + 4096u;
constexpr unsigned L_CSP = SCR + 8192u;
constexpr unsigned L_RA = SCR + 10240u;
constexpr unsigned L_TCH = SCR + 11264u;
constexpr unsigned LDS_BYTES = 163840u;
constexpr unsigned RB(int s) { return (unsigned)(s % 9) * 16384u; }

#define SPIN_CAP 20000
enum { ID_F = 100, ID_A = 200, ID_STE = 300, ID_STL = 301, ID_G = 310, ID_P1 = 320, ID_P2 = 321, ID_P3 = 322, ID_PAR = 330, ID_X = 340, ID_T = 350, ID_M = 399 };
#define CNT "\n\t.set vmc, vmc+1\n\t.set vmk%c[id], vmc"

DI i32x4 mk_rsrc(const void* p, unsigned bytes) {
  unsigned long long a = (unsigned long long)p;
  i32x4 r;
  r[0] = (int)(unsigned)a; r[1] = (int)((unsigned)(a >> 32) & 0xffffu); r[2] = (int)bytes; r[3] = 0x00020000;
  return r;
}
template <int ID, unsigned SOFF, unsigned LOFF> DI void glds(i32x4 rs, unsigned voff, unsigned sbase, unsigned ldsw) {
  unsigned t;
  asm volatile("s_add_u32 m0, %4, %6\n\ts_add_u32 %0, %3, %5\n\tbuffer_load_dwordx4 %1, %2, %0 offen lds" CNT
               : "=&s"(t) : "v"(voff), "s"(rs), "s"(sbase), "s"(ldsw), "i"(SOFF), "i"(LOFF), [id] "i"(ID) : "memory", "scc");
}
template <int ID> DI void touch(i32x4 rs, unsigned voff, unsigned soff) {
  asm volatile("s_mov_b32 m0, %3\n\ts_nop 0\n\tbuffer_load_dword %0, %1, %2 offen lds" CNT
               :: "v"(voff), "s"(rs), "s"(soff), "i"(L_TCH), [id] "i"(ID) : "memory");
}
template <int ID, int OFF> DI void ld4_sc1(u32x4& r, i32x4 rs, unsigned voff, unsigned soff) {
  asm volatile("buffer_load_dwordx4 %0, %1, %2, %3 offen offset:%c4 sc1" CNT : "=v"(r) : "v"(voff), "s"(rs), "s"(soff), "i"(OFF), [id] "i"(ID) : "memory");
}
template <int ID> DI void ld1_sc1(unsigned& r, i32x4 rs, unsigned voff, unsigned soff) {
  asm volatile("buffer_load_dword %0, %1, %2, %3 offen sc1" CNT : "=v"(r) : "v"(voff), "s"(rs), "s"(soff), [id] "i"(ID) : "memory");
}
template <int ID> DI void ld2(u32x2& r, i32x4 rs, unsigned voff, unsigned soff) {
  asm volatile("buffer_load_dwordx2 %0, %1, %2, %3 offen" CNT : "=v"(r) : "v"(voff), "s"(rs), "s"(soff), [id] "i"(ID) : "memory");
}
template <int ID> DI void ldh(unsigned& r, i32x4 rs, unsigned voff) {
  asm volatile("buffer_load_ushort %0, %1, %2, 0 offen" CNT : "=v"(r) : "v"(voff), "s"(rs), [id] "i"(ID) : "memory");
}
template <int ID, int OFF> DI void st4_sc1(u32x4 v, i32x4 rs, unsigned voff, unsigned soff) {
  asm volatile("buffer_store_dwordx4 %0, %1, %2, %3 offen offset:%c4 sc1\n\ts_nop 1" CNT :: "v"(v), "v"(voff), "s"(rs), "s"(soff), "i"(OFF), [id] "i"(ID) : "memory");
}
template <int ID> DI void st1_sc1(unsigned v, i32x4 rs, unsigned voff, unsigned soff) {
  asm volatile("buffer_store_dword %0, %1, %2, %3 offen sc1" CNT :: "v"(v), "v"(voff), "s"(rs), "s"(soff), [id] "i"(ID) : "memory");
}
#define WAITS ".if (vmc-vmk%c[id]) < 63\n\ts_waitcnt vmcnt(vmc-vmk%c[id])\n\t.endif"
template <int ID> DI void need() { asm volatile(WAITS :: [id] "i"(ID) : "memory"); }
template <int ID, class T> DI void need(T& a) { asm volatile(WAITS : "+v"(a) : [id] "i"(ID) : "memory"); }
template <int ID, class T> DI void need(T& a, T& b) { asm volatile(WAITS : "+v"(a), "+v"(b) : [id] "i"(ID) : "memory"); }
DI unsigned opq(unsigned v) { asm volatile("" : "+v"(v)); return v; }
template <class T> DI void pin(T& v) { asm volatile("" : "+v"(v)); }
DI void fence() { __builtin_amdgcn_sched_barrier(0); }
DI void bar_nolds() { fence(); asm volatile("s_barrier" ::: "memory"); fence(); }
DI void bar() { fence(); asm volatile("s_waitcnt lgkmcnt(0)\n\ts_barrier" ::: "memory"); fence(); }
DI u32x4 lds128(unsigned a) { return *(LDSP u32x4*)a; }
DI f32x4 lds128f(unsigned a) { return *(LDSP f32x4*)a; }
DI f32x2 lds64f(unsigned a) { return *(LDSP f32x2*)a; }
DI u32x4 ldstr(unsigned a0, unsigned a1) {
  s16x4 lo = __builtin_amdgcn_ds_read_tr16_b64_v4i16((LDSP s16x4*)a0);
  s16x4 hi = __builtin_amdgcn_ds_read_tr16_b64_v4i16((LDSP s16x4*)a1);
  return __builtin_bit_cast(u32x4, __builtin_shufflevector(lo, hi, 0, 1, 2, 3, 4, 5, 6, 7));
}
DI f32x16 mfma(u32x4 a, u32x4 b, f32x16 c) {
  return __builtin_amdgcn_mfma_f32_32x32x16_bf16(__builtin_bit_cast(bf16x8, a), __builtin_bit_cast(bf16x8, b), c, 0, 0, 0);
}
DI f32x4 mfma16(u32x4 a, u32x4 b, f32x4 c) {
  return __builtin_amdgcn_mfma_f32_16x16x32_bf16(__builtin_bit_cast(bf16x8, a), __builtin_bit_cast(bf16x8, b), c, 0, 0, 0);
}
DI unsigned pk(float lo, float hi) {
  f32x2 p = {lo, hi};
  return __builtin_bit_cast(unsigned, __builtin_convertvector(p, bf16x2));
}
DI float bf2f(unsigned v) { return __uint_as_float(v << 16); }
DI float hi2f(unsigned v) { return __uint_as_float(v & 0xffff0000u); }
DI float halfsum(float a, float b) {
  auto r = __builtin_amdgcn_permlane32_swap(__float_as_uint(a), __float_as_uint(b), false, false);
  return __uint_as_float(r[0]) + __uint_as_float(r[1]);
}
DI float sum4(f32x4 v) { return (v[0] + v[1]) + (v[2] + v[3]); }
DI unsigned split_hl(float v) { const unsigned p = pk(v, v); return pk(v, v - hi2f(p)); }
DI u32x4 split_op(float v) { const unsigned p = pk(v, v); const float l = v - hi2f(p); return u32x4{p, pk(l, l), 0u, 0u}; }
DI unsigned tau(unsigned r) { return 16u * ((r >> 2) & 1u) + (r & 3u) + 4u * (r >> 3); }
DI unsigned swap23(unsigned r) { return (r & 0x13u) | ((r & 4u) << 1) | ((r & 8u) >> 1); }
DI f32x2 cmul(f32x2 a, f32x2 b) { return f32x2{a[0] * b[0] - a[1] * b[1], a[0] * b[1] + a[1] * b[0]}; }
DI f32x2 cmad(f32x2 a, f32x2 b, f32x2 c) { return f32x2{a[0] * b[0] + (c[0] - a[1] * b[1]), a[0] * b[1] + (c[1] + a[1] * b[0])}; }
DI f32x2 csq(f32x2 a) { return f32x2{a[0] * a[0] - a[1] * a[1], 2.0f * a[0] * a[1]}; }
constexpr unsigned ONES = 0x3F803F80u;
template <int FROM, int TO> DI void rebase(unsigned& a) {
  constexpr int d = (int)(RB(TO) & ~0xffffu) - (int)(RB(FROM) & ~0xffffu);
  if constexpr (d != 0) asm volatile("v_add_u32 %0, %1, %0" : "+v"(a) : "i"(d));
}
constexpr unsigned RO(int s) { return RB(s) & 0xffffu; }

#define STAMP(i) do {} while (0)

template <int I, int N, class Fn> DI void static_for(Fn&& f) {
  if constexpr (I < N) { f(IC<I>{}); static_for<I + 1, N>(f); }
}

DI void poll_flags(unsigned v, bool sel, unsigned tag, i32x4 rs, unsigned voff, unsigned soff) {
  if (__all(!sel || v == tag)) return;
  for (int it = 0; it < SPIN_CAP; ++it) {
    __builtin_amdgcn_s_sleep(1);
    unsigned w;
    asm volatile("buffer_load_dword %0, %1, %2, %3 offen sc1\n\ts_waitcnt vmcnt(0)" : "=&v"(w) : "v"(voff), "s"(rs), "s"(soff) : "memory");
    if (__all(!sel || w == tag)) return;
  }
}

struct GluK { float k1, k2, iv; };
DI unsigned glu_pair(const f32x16& a0, const f32x16& a1, int e, const GluK& k) {
  float o[2];
#pragma unroll
  for (int t = 0; t < 2; ++t) {
    const float a = a0[e + t];
    const float z = a * (k.k1 + k.k2 * (a * a));
    const float s = __builtin_amdgcn_rcpf(__builtin_amdgcn_exp2f(z) * k.iv + k.iv);
    o[t] = (a * a1[e + t]) * s;
  }
  return pk(o[0], o[1]);
}

#define C_FLAGE 13
constexpr int DA = 8;
#define T2_G 6
#define T3_G 22
#define PRO 10
#define I_FLAGE 3
#define I_FLAGL 5
constexpr int sched_m(int n) {
  int m = PRO;
  for (int t = 0; t < 64; ++t) {
    const int c = t >> 2, i = t & 3, sg = m >> 1;
    const int cf = i >= 2 ? c - 1 : c - 2;
    const int freed = cf < 8 ? 2 * cf + 1 : 8 + cf;
    bool go = m < 64 && sg <= (freed < -1 ? -1 : freed) + 9;
    if (sg >= 16 && sg < 24 && i == 1) go = false;
    if (t == n) return go ? m : -1;
    if (go) ++m;
  }
  return m;
}
static_assert(sched_m(64) == 64, "ring prefetch incomplete");
constexpr int issued_before(int n) { int m = PRO; for (int t = 0; t < n; ++t) if (sched_m(t) >= 0) ++m; return m; }

struct Args { const u16 *x, *lre, *lim, *Bre, *Bim, *Cre, *Cim, *ldt, *Dsk, *wg, *wu, *wd; float* out; char* ws; };

template <bool SSM>
DI void body(const Args& p, const unsigned rt, const unsigned s, unsigned* stamp) {
  constexpr int Z = SSM ? 1000 : 0;
  asm volatile(".set vmc, 0");
  const unsigned lane = threadIdx.x & 63u;
  const unsigned wv = __builtin_amdgcn_readfirstlane(threadIdx.x >> 6);
  const unsigned j = s >> 1, r = lane & 31u, h = lane >> 5, cg = wv >> 2, rg = wv & 3u;

  const i32x4 rsX = mk_rsrc(p.x, SEQ * DM * 2), rsG = mk_rsrc(p.wg, DM * FF * 2), rsU = mk_rsrc(p.wu, DM * FF * 2);
  const i32x4 rsB = mk_rsrc(cg ? p.Bim : p.Bre, DM * DM * 2);
  const i32x4 rsWD = mk_rsrc(p.wd, FF * DM * 2), rsCr = mk_rsrc(p.Cre, DM * DM * 2), rsCi = mk_rsrc(p.Cim, DM * DM * 2);
  const i32x4 rsWS = mk_rsrc(p.ws, WS_END);
  const unsigned ldsw = wv * 1024u;
  const unsigned row = 8u * wv + (lane >> 3), cp = lane & 7u;
  const unsigned sw = (cp ^ ((row >> 1) & 7u)) * 16u;
  const unsigned hs = (((cp >> 2) ^ ((row >> 1) & 1u)) * 4u + (cp & 3u)) * 16u;
  const unsigned vX = ((row & 32u) + tau(row & 31u)) * 1024u + sw;
  const unsigned vB = swap23(row & 31u) * 1024u + sw;
  const unsigned vG = row * (FF * 2u) + hs;
  const unsigned pairB = SSM ? 16u + (j & 3u) + 3u * (j >> 2) : 8u + j;
  const bool hasB = !(SSM && (j & 3u) == 3u);
  const unsigned sX = rt * (128u * 1024u);
  const unsigned sWB = hasB ? pairB * 128u : SOOB;
  unsigned vWD, vC;

  const unsigned sJ = j * 65536u, sK = j * 128u;
  auto fillh = [&](auto MM) {
    constexpr int S = MM >> 1;
    constexpr unsigned T = MM & 1, L = RB(S) + T * 8192u;
    constexpr int ID = Z + ID_F + S;
    if constexpr (S < 24) {
      constexpr unsigned ch = S < 16 ? S >> 1 : S - 16;
      if constexpr (S < 16 && (S & 1) == 0) glds<ID, ch * 128u + T * 65536u, L>(rsX, vX, sX, ldsw);
      else if constexpr (S < 16 && SSM) glds<ID, ch * 128u + T * 32768u, L>(rsB, vB, sJ, ldsw);
      else glds<ID, ch * (64u * FF * 2u), L>(T ? rsU : rsG, vG, S < 16 ? sK : sWB, ldsw);
    } else {
      constexpr unsigned i = S - 24;
      if constexpr (i >= 8 && i < 16) glds<ID, (i - 8u) * 128u, L>(T ? rsCi : rsCr, vC, sJ, ldsw);
      else glds<ID, (i < 8 ? i : i - 8u) * (128u * DM * 2u) + T * 65536u, L>(rsWD, vWD, sK, ldsw);
    }
  };

  unsigned eR, eC, plr, pli, pdt;
  asm volatile("s_nop 4" ::: "memory");
  if constexpr (SSM) {
    const unsigned po = (64u * j + 32u * cg + swap23(r)) * 2u;
    ldh<Z + ID_PAR>(plr, mk_rsrc(p.lre, 1024), po); ldh<Z + ID_PAR>(pli, mk_rsrc(p.lim, 1024), po); ldh<Z + ID_PAR>(pdt, mk_rsrc(p.ldt, 1024), po);
  }
  {
    const unsigned l8 = lane < 8u ? lane : 0x00ffffffu;
    touch<Z + ID_T>(rsX, l8 * 128u, sX + (s * 8u + wv) * 1024u);
    if constexpr (SSM) touch<Z + ID_T>(rsB, l8 * 128u, sJ + (rt * 4u + rg) * 1024u);
    else touch<Z + ID_T>(cg ? rsU : rsG, lane < 8u ? lane * (FF * 2u) : OOB, sK + (rt * 32u + rg * 8u) * (FF * 2u));
    unsigned long long pc;
    unsigned span;
    asm volatile("s_getpc_b64 %0\n.Ls5beg%c2:\n\ts_mov_b32 %1, .Ls5end%c2-.Ls5beg%c2" : "=s"(pc), "=s"(span) : "i"(Z));
    touch<Z + ID_T>(mk_rsrc((const void*)(pc & ~127ull), span), lane < 2u ? lane * 16384u : OOB, (((blockIdx.x >> 6) * 4u + (rt & 3u)) * 8u + wv) * 128u);
  }
  static_for<0, PRO>(fillh);
  ld1_sc1<Z + ID_M>(eR, rsWS, 0u, WS_EROW + rt * 128u);
  if constexpr (SSM) ld1_sc1<Z + ID_M>(eC, rsWS, 0u, WS_ECOL + j * 128u);

  f32x2 lam, cf;
  if constexpr (SSM) {
    asm volatile(WAITS : "+v"(plr), "+v"(pli), "+v"(pdt) : [id] "i"(Z + ID_PAR) : "memory");
    const float dt = bf2f(pk(expf(bf2f(pdt)), 0.f) & 0xffffu);
    const float Lr = bf2f(plr), Li = bf2f(pli);
    const float e = expf(dt * Lr);
    float sn, cs;
    sincosf(dt * Li, &sn, &cs);
    lam = f32x2{e * cs, e * sn};
    const float nr = lam[0] - 1.0f, inv = 2.0f / (Lr * Lr + Li * Li);
    cf = f32x2{(nr * Lr + lam[1] * Li) * inv, (lam[1] * Lr - nr * Li) * inv};
    pin(lam); pin(cf);
  }

  const unsigned rot = rg ^ (2u * cg);
  unsigned xa[4], wa[4];
  auto ga_init = [&](int i, unsigned win, unsigned ln) {
    const unsigned q4 = (ln & 15u) >> 2;
    return win + 128u * (16u * (i ^ rot) + 8u * (ln >> 5) + q4) + 64u * (cg ^ (q4 >> 1)) + 32u * ((ln >> 4) & 1u) + 8u * (ln & 3u);
  };
#pragma unroll
  for (int i = 0; i < 4; ++i) {
    const unsigned t = 128u * r + 16u * ((2u * (i ^ rot) + h) ^ ((r >> 1) & 7u));
    xa[i] = t + rg * 4096u;
    wa[i] = SSM ? t + cg * 8192u : ga_init(i, 0u, lane);
  }
  u32x4 xf[32], w0[2], w1[2];
  auto reads = [&](auto NN, auto MM) {
    constexpr int N = NN, M = MM, C = N >> 2, i = N & 3;
    constexpr int SW = C < 8 ? 2 * C + 1 : 8 + C;
    constexpr int SP = C == 0 ? SW : (C <= 8 ? 2 * C - 1 : 7 + C);
    if constexpr (M == 0) {
      if constexpr (C < 8) { rebase<(C == 0 ? 0 : 2 * C - 2), 2 * C>(xa[i]); xf[N] = lds128(xa[i] + RO(2 * C)); }
      if constexpr (SSM && C == 8) wa[i] = ga_init(i, RB(SW) & ~0xffffu, opq(lane)); else rebase<SP, SW>(wa[i]);
      if constexpr (SSM && C < 8) w0[N & 1] = lds128(wa[i] + RO(SW)); else w0[N & 1] = ldstr(wa[i] + RO(SW), wa[i] + RO(SW) + 512u);
    } else {
      if constexpr (SSM && C < 8) w1[N & 1] = lds128(wa[i] + RO(SW) + 4096u); else w1[N & 1] = ldstr(wa[i] + RO(SW) + 8192u, wa[i] + RO(SW) + 8704u);
    }
  };

  float sx0 = 0.f, sx1 = 0.f, sq0 = 0.f, sq1 = 0.f, mean = 0.f;
  f32x4 cs4 = {};
  u32x4 sel0, sel1;
  auto mksel = [&](unsigned ln, unsigned row, u32x4& sel) {
    const unsigned t = (ln & 15u) == row + ((ln >> 2) & 4u) ? ONES : 0u;
    sel = u32x4{t, t, t, t}; pin(sel);
  };
  mksel(lane, 0u, sel0); mksel(lane, 8u, sel1);
  GluK gk;
  unsigned opM = 0u, tagR = 0u, tagC = 0u, poll;
  f32x16 a0 = {}, a1 = {}, b0 = {}, b1 = {};
  f32x4 st4, ca4, cb4, gt, gp;
  f32x2 e1 = {0.f, 0.f}, pre = {0.f, 0.f}, l16, wseg, wj, l1k, p2, p4, sg[8];
  u32x4 o0, o1, g0, g1, opA, opB;
  const unsigned hrow = (rt * 4u + rg) * NQ;
  const unsigned gso = WS_AGG + (2u * j + cg) * 8192u;
  const unsigned fso = WS_CTL + rt * 256u;
  const unsigned lane0 = lane;
  const f32x2 one = {1.f, 0.f};

  need<Z + ID_F + 1>();
  bar();
  STAMP(1);
  reads(IC<0>{}, IC<0>{}); reads(IC<0>{}, IC<1>{}); reads(IC<1>{}, IC<0>{}); reads(IC<1>{}, IC<1>{});
  static_for<0, 128>([&](auto GG) {
    constexpr int G = GG, N = G >> 1, M = G & 1, C = N >> 2, i = N & 3;
    fence();
    {
      const u32x4 x = xf[N & 31], w = M ? w1[N & 1] : w0[N & 1];
      f32x16& acc = C >= 8 ? (M ? b1 : b0) : (M ? a1 : a0);
      if constexpr (SSM && C < 8) acc = mfma(x, w, acc); else acc = mfma(w, x, acc);
      if constexpr (i == (C == 8 ? 3 : 0)) cs4 = mfma16(M ? sel1 : sel0, w, cs4);
      if constexpr (G == 63 || G == 127) {
        const u32x4 m = {opM, opM, 0u, 0u};
        if constexpr (G == 127) { b0 = mfma(opA, m, b0); b1 = mfma(opB, m, b1); }
        else if constexpr (SSM) { a0 = mfma(m, opA, a0); a1 = mfma(m, opB, a1); }
        else { a0 = mfma(opA, m, a0); a1 = mfma(opB, m, a1); }
      }
    }
    fence();
    constexpr bool USE_LANE = (i == 2 && M == 0 && (C == 7 || C >= 9)) || G == 62 || (G >= 64 && G <= 92);
    const unsigned lane = USE_LANE ? opq(lane0) : lane0, r = lane & 31u, h = lane >> 5, j1 = 2u * rg + h, j2 = j1 + 8u;
    if constexpr (i == 2 && M == 0) {
      const bool leader = wv == 0u && lane == 0u;
      if constexpr (C == 7) *(LDSP float*)(L_STP + (rg * 32u + r) * 16u + (cg * 2u + h) * 4u) = halfsum(sx0 + sx1, sq0 + sq1);
      if constexpr (C == 7 || C == 15) { *(LDSP float*)(L_CSP + ((cg * 2u + h) * 32u + r) * 16u + rg * 4u) = cs4[0]; cs4 = f32x4{}; }
      if constexpr (SSM && C == 9) *(LDSP f32x2*)(L_SEG + ((cg * 8u + j1) * 32u + r) * 8u) = e1;
      if constexpr (SSM && C == 15) {
        need<Z + ID_G>(g0, g1);
        bool ok = (j1 >= rt || (g0[1] == tagC && g0[3] == tagC)) && (j2 >= rt || (g1[1] == tagC && g1[3] == tagC));
        if (!__all(ok)) {
          const unsigned gv1 = j1 < rt ? (j1 * 32u + r) * 16u : OOB, gv2 = j2 < rt ? (j2 * 32u + r) * 16u : OOB;
          for (int it = 0; it < SPIN_CAP; ++it) {
            __builtin_amdgcn_s_sleep(1);
            asm volatile("buffer_load_dwordx4 %0, %2, %4, %5 offen sc1\n\tbuffer_load_dwordx4 %1, %3, %4, %5 offen sc1\n\ts_waitcnt vmcnt(0)"
                         : "=&v"(g0), "=&v"(g1) : "v"(gv1), "v"(gv2), "s"(rsWS), "s"(gso) : "memory");
            ok = (j1 >= rt || (g0[1] == tagC && g0[3] == tagC)) && (j2 >= rt || (g1[1] == tagC && g1[3] == tagC));
            if (__all(ok)) break;
          }
        }
        const f32x2 t = cmul(wj, cmad(l1k, f32x2{__uint_as_float(g0[0]), __uint_as_float(g0[2])}, f32x2{__uint_as_float(g1[0]), __uint_as_float(g1[2])}));
        *(LDSP float*)(L_CAR + ((cg * 32u + r) * 4u + rg) * 8u + h * 4u) = halfsum(t[0], t[1]);
      }
      static_assert(issued_before(N) >= 2 * (C < 7 ? 2 * C + 3 : 9 + C) + 2, "slot not issued yet");
      need<Z + ID_F + (C < 7 ? 2 * C + 3 : 9 + C)>();
      if constexpr (!SSM && C == C_FLAGE) need<Z + ID_STE>();
      if constexpr (C == 6) { if constexpr (SSM) need<Z + ID_M>(eR, eC); else need<Z + ID_M>(eR); tagR = eR + 1u; if constexpr (SSM) tagC = eC + 1u; }
      if constexpr (C == 7 || C == 15 || (SSM && C == 9)) bar(); else bar_nolds();
      if constexpr (C == 7) st4 = lds128f(L_STP + (rg * 32u + r) * 16u);
      if constexpr (C == 7 || C == 15) { ca4 = lds128f(L_CSP + (cg * 64u + r) * 16u); cb4 = lds128f(L_CSP + (cg * 64u + 32u + r) * 16u); }
      if constexpr (SSM && C == 9) {
#pragma unroll
        for (int t = 0; t < 8; ++t) sg[t] = lds64f(L_SEG + ((cg * 8u + t) * 32u + r) * 8u);
      }
      if constexpr (SSM && C == 15) { g0 = lds128(L_CAR + (cg * 32u + r) * 32u); g1 = lds128(L_CAR + (cg * 32u + r) * 32u + 16u); }
      if constexpr (C == 9) {
        const unsigned row = 8u * wv + (lane >> 3), cp = lane & 7u;
        vWD = row * 1024u + (((cp >> 2) ^ ((row >> 1) & 1u)) * 4u + (cp & 3u)) * 16u;
      }
      if constexpr (!SSM && C == C_FLAGE) st1_sc1<Z + ID_M>(tagR, rsWS, leader ? s * 4u : OOB, fso);
      if constexpr (SSM && C == 14) {
        ld4_sc1<Z + ID_G, 0>(g0, rsWS, j1 < rt ? (j1 * 32u + r) * 16u : OOB, gso);
        ld4_sc1<Z + ID_G, 0>(g1, rsWS, j2 < rt ? (j2 * 32u + r) * 16u : OOB, gso);
      }
      if constexpr (C == 15) ld1_sc1<Z + ID_P1>(poll, rsWS, r * 4u, fso);
      if constexpr (SSM && C == 15) st1_sc1<Z + ID_M>(tagC, rsWS, (rt == 15u && leader) ? j * 128u : OOB, WS_ECOL);
      if constexpr (C == 7) STAMP(2);
    }
    if constexpr (N + 2 < 64) reads(IC<(N + 2 < 64 ? N + 2 : 0)>{}, IC<M>{});
    if constexpr (M == 1 && sched_m(N) >= 0) { static_assert(sched_m(N) < 48 || N >= 38, "vWD"); fillh(IC<(sched_m(N) >= 0 ? sched_m(N) : 0)>{}); }
    if constexpr (G == T2_G) touch<Z + ID_T>(cg ? rsU : rsG, lane < 8u ? lane * (FF * 2u) : OOB, sWB + (rt * 32u + rg * 8u) * (FF * 2u));
    if constexpr (G == T3_G) {
      const unsigned w = (rt * 2u + (s & 1u)) * 8u + wv;
      touch<Z + ID_T>(rsWD, lane < 6u ? lane * (256u * 1024u) : OOB, sK + w * 1024u);
      touch<Z + ID_T>((w & 1u) ? rsCi : rsCr, lane < 4u ? lane * 128u : OOB, sJ + (w >> 1) * 512u);
    }
    if constexpr (G < 60) {
      constexpr int D0 = C < 7 ? G & 7 : 2 * (G & 7), D1 = C < 7 ? D0 + 1 : D0 + 2;
#pragma unroll
      for (int d = D0; d < D1; ++d) {
        const unsigned v = xf[4 * C + (d >> 2)][d & 3];
        const float lo = bf2f(v), hi = hi2f(v);
        sx0 += lo; sx1 += hi; sq0 = fmaf(lo, lo, sq0); sq1 = fmaf(hi, hi, sq1);
      }
      pin(sx0); pin(sx1); pin(sq0); pin(sq1);
    }
    if constexpr (G == 69) mksel(lane, 0u, sel0);
    if constexpr (G == 70) mksel(lane, 8u, sel1);
    if constexpr (G == 61) {
      mean = (st4[0] + st4[2]) * (1.0f / DM);
      gk.iv = fmaxf((st4[1] + st4[3]) * (1.0f / DM) - mean * mean, 0.0f) + 1e-5f;
      pin(mean); pin(gk.iv);
    }
    if constexpr (G == 61 || G == 125) { opA = split_op(sum4(ca4)); pin(opA); }
    if constexpr (G == 62 || G == 126) { opB = split_op(sum4(cb4)); pin(opB); }
    if constexpr (G == 62) { opM = h ? 0u : split_hl(-mean); pin(opM); }
    if constexpr (G == 64) {
      const float ra = __builtin_amdgcn_rsqf(gk.iv);
      *(LDSP float*)(L_RA + h * 512u + (rg * 32u + tau(r)) * 4u) = h ? -mean * ra : ra;
      asm volatile("" ::: "memory");
      gk.k1 = -2.302208198f * ra;
      gk.k2 = -0.1029432397f * (ra * ra * ra);
      pin(gk.k1); pin(gk.k2);
    }
    if constexpr (!SSM && G >= 66 && G < 82) {
      constexpr int E = (G - 66) & ~3, ST = (G - 66) & 3;
#pragma unroll
      for (int t = 0; t < 4; ++t) {
        const float a = a0[E + t];
        if constexpr (ST == 0) gt[t] = gk.k1 + gk.k2 * (a * a);
        if constexpr (ST == 1) gt[t] = __builtin_amdgcn_exp2f(a * gt[t]);
        if constexpr (ST == 2) gt[t] = __builtin_amdgcn_rcpf(gt[t] * gk.iv + gk.iv);
        if constexpr (ST == 3) gt[t] *= a * a1[E + t];
      }
      if constexpr (ST == 3) {
        u32x4& o = E < 8 ? o0 : o1;
        o[(E & 4) / 2] = pk(gt[0], gt[1]); o[(E & 4) / 2 + 1] = pk(gt[2], gt[3]);
        if constexpr (E == 4) st4_sc1<Z + ID_STE, 0>(o0, rsWS, lane * 16u, WS_H + (hrow + 2u * j + cg) * 2048u);
        else if constexpr (E == 12) st4_sc1<Z + ID_STE, 1024>(o1, rsWS, lane * 16u, WS_H + (hrow + 2u * j + cg) * 2048u);
        else pin(o);
      } else pin(gt);
    }
    if constexpr (SSM) {
      if constexpr (G >= 67 && G < 75) {
        constexpr int T = 2 * (G - 67);
        const f32x4 rs = (T & 4) ? gp : st4;
#pragma unroll
        for (int t = T; t < T + 2; ++t) {
          a0[t] *= rs[t & 3]; a1[t] *= rs[t & 3];
          e1 = cmad(lam, e1, f32x2{a0[t], a1[t]});
        }
        pin(e1);
      }
      if constexpr (G == 65 || G == 68) st4 = lds128f(L_RA + (rg * 32u + 16u * h + (G == 65 ? 0u : 8u)) * 4u);
      if constexpr (G == 65 || G == 70) gp = lds128f(L_RA + (rg * 32u + 16u * h + (G == 65 ? 4u : 12u)) * 4u);
      if constexpr (G == 65) { l16 = csq(csq(lam)); pin(l16); }
      if constexpr (G == 66) { l16 = csq(csq(l16)); pin(l16); }
      if constexpr (G == 77) e1 = f32x2{0.f, 0.f};
      if constexpr (G >= 77 && G < 81) {
#pragma unroll
        for (int t = 2 * (G - 77); t < 2 * (G - 77) + 2; ++t) { if (t) pre = (unsigned)t == j1 ? e1 : pre; e1 = cmad(l16, e1, sg[t]); }
        pin(pre); pin(e1);
        if constexpr (G == 80) {
          st4_sc1<Z + ID_M, 0>(u32x4{__float_as_uint(e1[0]), tagC, __float_as_uint(e1[1]), tagC}, rsWS, (rg == 0u && h == 0u) ? (rt * 32u + r) * 16u : OOB, gso);
          STAMP(3);
        }
      }
      if constexpr (G == 82) { p2 = csq(l16); p4 = csq(p2); l1k = csq(p4); pin(p2); pin(p4); pin(l1k); }
      if constexpr (G == 83) { wseg = cmul((j1 & 1u) ? l16 : one, (j1 & 2u) ? p2 : one); pin(wseg); }
      if constexpr (G == 85) { wseg = cmul(wseg, (j1 & 4u) ? p4 : one); pin(wseg); }
      if constexpr (G == 86) { p2 = csq(l1k); p4 = csq(p2); pin(p2); pin(p4); }
      if constexpr (G == 87 || G == 88) {
        const unsigned e = j1 < rt ? rt - 1u - (j2 < rt ? j2 : j1) : 0u;
        if constexpr (G == 87) wj = cmul((e & 1u) ? l1k : one, (e & 2u) ? p2 : one);
        else wj = cmul(wj, (e & 4u) ? p4 : one);
        pin(wj);
      }
      if constexpr (G == 89) { l1k = j2 < rt ? csq(p4) : one; pin(l1k); }
    }
  });
  fence();
  STAMP(4);

  {
  const unsigned lane = opq(lane0), r = lane & 31u, h = lane >> 5, q4 = (lane & 15u) >> 2, avo = lane * 16u, fvo = r * 4u;
  const unsigned tid = wv * 64u + lane;
  const bool leader = wv == 0u && lane == 0u;
  const unsigned kg = rg, rh2 = cg, rh = s & 1u;
  const unsigned aso = WS_H + ((rt * 4u + rh * 2u + rh2) * NQ + kg) * 2048u;
  u32x4 A[19][2];
  auto lda = [&](auto II) { constexpr int I = II; ld4_sc1<Z + ID_A + I, 0>(A[I][0], rsWS, avo, aso + I * 8192u); ld4_sc1<Z + ID_A + I, 1024>(A[I][1], rsWS, avo, aso + I * 8192u); };
  need<Z + ID_P1>(poll);
  poll_flags(poll, r < 16u && (r & 1u), tagR, rsWS, fvo, fso);
  static_for<0, 4>(lda);
  fence();
  STAMP(5);
  if constexpr (SSM) {
    const f32x2 cin = {(__uint_as_float(g0[0]) + __uint_as_float(g0[2])) + (__uint_as_float(g1[0]) + __uint_as_float(g1[2])),
                       (__uint_as_float(g0[1]) + __uint_as_float(g0[3])) + (__uint_as_float(g1[1]) + __uint_as_float(g1[3]))};
    f32x2 e = cmad(wseg, cin, pre);
    unsigned pr_[8], pi_[8];
#pragma unroll
    for (int t = 0; t < 8; ++t) {
      e = cmad(lam, e, f32x2{a0[2 * t], a1[2 * t]});
      const f32x2 y0 = cmul(cf, e);
      e = cmad(lam, e, f32x2{a0[2 * t + 1], a1[2 * t + 1]});
      const f32x2 y1 = cmul(cf, e);
      pr_[t] = pk(y0[0], y1[0]); pi_[t] = pk(-y0[1], -y1[1]);
    }
    u32x4 id[2];
#pragma unroll
    for (int ks = 0; ks < 2; ++ks)
#pragma unroll
      for (int q = 0; q < 4; ++q) {
        const int jj = 8 * ks + 2 * q;
        const unsigned r0 = (jj & 3) + 8 * (jj >> 2) + 4u * h;
        id[ks][q] = r == r0 ? 0x3F80u : (r == r0 + 1u ? 0x3F800000u : 0u);
      }
    f32x16 zr = mfma(u32x4{pr_[0], pr_[1], pr_[2], pr_[3]}, id[0], f32x16{});
    f32x16 zi = mfma(u32x4{pi_[0], pi_[1], pi_[2], pi_[3]}, id[0], f32x16{});
    zr = mfma(u32x4{pr_[4], pr_[5], pr_[6], pr_[7]}, id[1], zr);
    zi = mfma(u32x4{pi_[4], pi_[5], pi_[6], pi_[7]}, id[1], zi);
    u32x4 o[4];
#pragma unroll
    for (int q = 0; q < 4; ++q) {
      o[0][q] = pk(zr[2 * q], zr[2 * q + 1]); o[1][q] = pk(zr[8 + 2 * q], zr[9 + 2 * q]);
      o[2][q] = pk(zi[2 * q], zi[2 * q + 1]); o[3][q] = pk(zi[8 + 2 * q], zi[9 + 2 * q]);
    }
    const unsigned so = WS_H + (hrow + 32u + 4u * j + cg) * 2048u;
    st4_sc1<Z + ID_STE, 0>(o[0], rsWS, avo, so); st4_sc1<Z + ID_STE, 1024>(o[1], rsWS, avo, so);
    st4_sc1<Z + ID_STE, 0>(o[2], rsWS, avo, so + 4096u); st4_sc1<Z + ID_STE, 1024>(o[3], rsWS, avo, so + 4096u);
    fence();
  }
  STAMP(6);
  {
#pragma unroll
    for (int e = 0; e < 4; ++e) o0[e] = glu_pair(b0, b1, 2 * e, gk);
#pragma unroll
    for (int e = 0; e < 4; ++e) o1[e] = glu_pair(b0, b1, 8 + 2 * e, gk);
    const unsigned un = 2u * pairB + cg;
    const unsigned so = (hasB && un < 43u) ? WS_H + (hrow + un + (SSM ? 32u : 0u)) * 2048u : SOOB;
    st4_sc1<Z + ID_STL, 0>(o0, rsWS, avo, so); st4_sc1<Z + ID_STL, 1024>(o1, rsWS, avo, so);
  }
  fence();
  STAMP(7);
  unsigned pa[2], ca[2];
#pragma unroll
  for (unsigned t = 0; t < 2; ++t) {
    pa[t] = (RB(24) & ~0xffffu) + 128u * (32u * kg + 4u * h + q4) + 64u * (t ^ (q4 >> 1)) + 32u * ((lane >> 4) & 1u) + 8u * (lane & 3u);
    ca[t] = (RB(32) & ~0xffffu) + (kg >> 1) * 8192u + 128u * r + 16u * ((4u * (kg & 1u) + 2u * t + h) ^ ((r >> 1) & 7u));
  }
  u32x4 bw[19][4];
  auto readsB = [&](auto II) {
    constexpr int I = II, S = 24 + I;
    if constexpr (I >= 8 && I < 16) {
      if constexpr (I > 8) { rebase<S - 1, S>(ca[0]); rebase<S - 1, S>(ca[1]); }
#pragma unroll
      for (int ks = 0; ks < 2; ++ks) { bw[I][2 * ks] = lds128(ca[ks] + RO(S)); bw[I][2 * ks + 1] = lds128(ca[ks] + RO(S) + 4096u); }
    } else {
      if constexpr (I > 0) { rebase<(I == 16 ? 31 : S - 1), S>(pa[0]); rebase<(I == 16 ? 31 : S - 1), S>(pa[1]); }
#pragma unroll
      for (int ks = 0; ks < 2; ++ks) {
        bw[I][2 * ks] = ldstr(pa[0] + RO(S) + 2048u * ks, pa[0] + RO(S) + 2048u * ks + 1024u);
        bw[I][2 * ks + 1] = ldstr(pa[1] + RO(S) + 2048u * ks, pa[1] + RO(S) + 2048u * ks + 1024u);
      }
    }
  };
  auto mm = [&](auto II) {
    constexpr int I = II;
    need<Z + ID_A + I>(A[I][0], A[I][1]);
    fence();
    if (I < 18 || kg != 3u) {
      a0 = mfma(bw[I][0], A[I][0], a0); a1 = mfma(bw[I][1], A[I][0], a1);
      a0 = mfma(bw[I][2], A[I][1], a0); a1 = mfma(bw[I][3], A[I][1], a1);
    }
    fence();
  };
  a0 = f32x16{}; a1 = f32x16{};
  u32x2 xv[2], dv;
  const unsigned orow = rh * 64u + tau(tid >> 4), ocol = j * 64u + (tid & 15u) * 4u;
  {
    const unsigned row = 8u * wv + (lane >> 3), cp = lane & 7u;
    vC = row * 1024u + (cp ^ ((row >> 1) & 7u)) * 16u;
  }
  readsB(IC<0>{});
  static_for<0, 10>([&](auto II) {
    constexpr int I = II;
    fence();
    if constexpr (I < 9) need<Z + ID_F + 25 + I>();
    if constexpr (SSM && I == I_FLAGE) need<Z + ID_STE>();
    if constexpr (I == I_FLAGL) need<Z + ID_STL>();
    bar();
    if constexpr (SSM && I == I_FLAGE) st1_sc1<Z + ID_M>(tagR, rsWS, leader ? s * 4u : OOB, fso);
    if constexpr (I == I_FLAGL) { st1_sc1<Z + ID_M>(tagR, rsWS, leader ? s * 4u : OOB, fso + 64u); STAMP(8); }
    if constexpr (I < 9) readsB(IC<(I < 9 ? I + 1 : 0)>{});
    fence();
    if constexpr (I == 0) { fillh(IC<64>{}); fillh(IC<65>{}); }
    fillh(IC<2 * (33 + I)>{}); fillh(IC<2 * (33 + I) + 1>{});
    if constexpr (I < 4) mm(II);
  });
  ld2<Z + ID_X>(xv[0], rsX, orow * 1024u + ocol * 2u, sX); ld2<Z + ID_X>(xv[1], rsX, orow * 1024u + ocol * 2u, sX + 32768u);
  ld2<Z + ID_X>(dv, mk_rsrc(p.Dsk, 1024), ocol * 2u, 0u);
  ld1_sc1<Z + ID_P2>(poll, rsWS, fvo, fso);
  need<Z + ID_P2>(poll);
  bar();
  poll_flags(poll, ((r >> 4) ^ r ^ 1u) & 1u, tagR, rsWS, fvo, fso);
  STAMP(9);
  ld1_sc1<Z + ID_P3>(poll, rsWS, fvo, fso);
  static_for<4, 4 + DA>(lda);
  static_for<4, 19>([&](auto II) {
    constexpr int I = II;
    if constexpr (I + DA == 16) {
      need<Z + ID_P3>(poll);
      poll_flags(poll, r >= 16u, tagR, rsWS, fvo, fso);
      st1_sc1<Z + ID_M>(tagR, rsWS, (s == 0u && leader) ? rt * 128u : OOB, WS_EROW);
    }
    if constexpr (I + 2 >= 10 && I + 2 <= 18) readsB(IC<(I + 2 >= 10 && I + 2 <= 18 ? I + 2 : 0)>{});
    mm(II);
    if constexpr (I + DA <= 18) lda(IC<(I + DA <= 18 ? I + DA : 0)>{});
  });
  STAMP(10);
  bar();
#pragma unroll
  for (int g = 0; g < 4; ++g) {
    const unsigned a = ((kg * 64u + rh2 * 32u + r) * 68u + 8u * g + 4u * h) * 4u;
    *(LDSP f32x4*)a = f32x4{a0[4 * g], a0[4 * g + 1], a0[4 * g + 2], a0[4 * g + 3]};
    *(LDSP f32x4*)(a + 128u) = f32x4{a1[4 * g], a1[4 * g + 1], a1[4 * g + 2], a1[4 * g + 3]};
  }
  fence();
  asm volatile(WAITS "\n.Ls5end%c[z]:" : "+v"(xv[0]), "+v"(xv[1]), "+v"(dv) : [id] "i"(Z + ID_X), [z] "i"(Z) : "memory");
  bar();
#pragma unroll
  for (unsigned m = 0; m < 2; ++m) {
    const unsigned a = ((m * 32u + (tid >> 4)) * 68u + (tid & 15u) * 4u) * 4u;
    const f32x4 pt = (lds128f(a) + lds128f(a + 17408u)) + (lds128f(a + 34816u) + lds128f(a + 52224u));
    const f32x2 ab = {*(LDSP float*)(L_RA + (orow + 32u * m) * 4u), *(LDSP float*)(L_RA + 512u + (orow + 32u * m) * 4u)};
    f32x4 o;
#pragma unroll
    for (int e = 0; e < 4; ++e) {
      const float xe = (e & 1) ? hi2f(xv[m][e >> 1]) : bf2f(xv[m][e >> 1]);
      const float de = (e & 1) ? hi2f(dv[e >> 1]) : bf2f(dv[e >> 1]);
      o[e] = pt[e] + xe + (xe * ab[0] + ab[1]) * de;
    }
    *(f32x4*)(p.out + (size_t)(rt * 128u + orow + 32u * m) * DM + ocol) = o;
  }
  }
}

#if defined(__HIP_DEVICE_COMPILE__)
__attribute__((target("no-packed-fp32-ops")))
#endif
__global__ __launch_bounds__(512, 2) void s5_geglu_kernel(const Args p) {
  __shared__ __attribute__((aligned(16))) char smem[LDS_BYTES];
  unsigned stamp[32] = {};
  STAMP(0);
  asm volatile("" :: "v"((unsigned)(uintptr_t)(LDSP char*)smem));
  const unsigned b = blockIdx.x, xcd = b & 7u, idx = b >> 3;
  const unsigned rt = (xcd >> 1) * 4u + (idx & 3u), s = (xcd & 1u) * 8u + (idx >> 2);
  if ((s & 1u) == 0u) body<true>(p, rt, s, stamp);
  else body<false>(p, rt, s, stamp);
}

extern "C" void kernel_launch(void* const* d_in, const int* in_sizes, int n_in, void* d_out, int out_size, void* d_ws,
                              size_t ws_size, hipStream_t stream) {
  Args a;
  a.x = (const u16*)d_in[0]; a.lre = (const u16*)d_in[1]; a.lim = (const u16*)d_in[2]; a.Bre = (const u16*)d_in[3];
  a.Bim = (const u16*)d_in[4]; a.Cre = (const u16*)d_in[5]; a.Cim = (const u16*)d_in[6]; a.ldt = (const u16*)d_in[7];
  a.Dsk = (const u16*)d_in[8]; a.wg = (const u16*)d_in[10]; a.wu = (const u16*)d_in[11]; a.wd = (const u16*)d_in[12];
  a.out = (float*)d_out; a.ws = (char*)d_ws;
  s5_geglu_kernel<<<256, 512, 0, stream>>>(a);
}
```

```cpp
#define PROBE 0
#define PROBE_SSM false
#include <hip/hip_runtime.h>
#include <stdint.h>
#include <type_traits>

typedef unsigned short u16;
typedef __attribute__((ext_vector_type(4))) short s16x4;
typedef __attribute__((ext_vector_type(8))) __bf16 bf16x8;
typedef __attribute__((ext_vector_type(2))) __bf16 bf16x2;
typedef __attribute__((ext_vector_type(4))) unsigned u32x4;
typedef __attribute__((ext_vector_type(2))) unsigned u32x2;
typedef __attribute__((ext_vector_type(4))) int i32x4;
typedef __attribute__((ext_vector_type(2))) float f32x2;
typedef __attribute__((ext_vector_type(4))) float f32x4;
typedef __attribute__((ext_vector_type(16))) float f32x16;
#define LDSP __attribute__((address_space(3)))
#define DI __device__ __forceinline__
template <int V> using IC = std::integral_constant<int, V>;

constexpr unsigned SEQ = 2048, DM = 512, FF = 1376;
constexpr unsigned NQ = 75;
constexpr unsigned WS_H = 0;
constexpr unsigned WS_AGG = 64u * NQ * 2048u;
constexpr unsigned WS_CTL = WS_AGG + 16u * 16u * 512u;
constexpr unsigned WS_EROW = WS_CTL + 4096u;
constexpr unsigned WS_ECOL = WS_CTL + 8192u;
constexpr unsigned WS_END = WS_CTL + 12288u;
constexpr unsigned OOB = 0x7ffffff0u;
constexpr unsigned SOOB = 0x40000000u;

constexpr unsigned SCR = 9u * 16384u;
constexpr unsigned L_STP = SCR;
constexpr unsigned L_CAR = SCR + 2048u;
constexpr unsigned L_SEG = SCR + 4096u;
constexpr unsigned L_CSP = SCR + 8192u;
constexpr unsigned L_RA = SCR + 10240u;
constexpr unsigned L_TCH = SCR + 11264u;
constexpr unsigned LDS_BYTES = 163840u;
constexpr unsigned RB(int s) { return (unsigned)(s % 9) * 16384u; }

#define SPIN_CAP 20000
enum { ID_F = 100, ID_A = 200, ID_STE = 300, ID_STL = 301, ID_G = 310, ID_P1 = 320, ID_P2 = 321, ID_P3 = 322, ID_PAR = 330, ID_X = 340, ID_T = 350, ID_M = 399 };
#define CNT "\n\t.set vmc, vmc+1\n\t.set vmk%c[id], vmc"

DI i32x4 mk_rsrc(const void* p, unsigned bytes) {
  unsigned long long a = (unsigned long long)p;
  i32x4 r;
  r[0] = (int)(unsigned)a; r[1] = (int)((unsigned)(a >> 32) & 0xffffu); r[2] = (int)bytes; r[3] = 0x00020000;
  return r;
}
template <int ID, unsigned SOFF, unsigned LOFF> DI void glds(i32x4 rs, unsigned voff, unsigned sbase, unsigned ldsw) {
  unsigned t;
  asm volatile("s_add_u32 m0, %4, %6\n\ts_add_u32 %0, %3, %5\n\tbuffer_load_dwordx4 %1, %2, %0 offen lds" CNT
               : "=&s"(t) : "v"(voff), "s"(rs), "s"(sbase), "s"(ldsw), "i"(SOFF), "i"(LOFF), [id] "i"(ID) : "memory", "scc");
}
template <int ID> DI void touch(i32x4 rs, unsigned voff, unsigned soff) {
  asm volatile("s_mov_b32 m0, %3\n\ts_nop 0\n\tbuffer_load_dword %0, %1, %2 offen lds" CNT
               :: "v"(voff), "s"(rs), "s"(soff), "i"(L_TCH), [id] "i"(ID) : "memory");
}
template <int ID, int OFF> DI void ld4_sc1(u32x4& r, i32x4 rs, unsigned voff, unsigned soff) {
  asm volatile("buffer_load_dwordx4 %0, %1, %2, %3 offen offset:%c4 sc1" CNT : "=v"(r) : "v"(voff), "s"(rs), "s"(soff), "i"(OFF), [id] "i"(ID) : "memory");
}
template <int ID> DI void ld1_sc1(unsigned& r, i32x4 rs, unsigned voff, unsigned soff) {
  asm volatile("buffer_load_dword %0, %1, %2, %3 offen sc1" CNT : "=v"(r) : "v"(voff), "s"(rs), "s"(soff), [id] "i"(ID) : "memory");
}
template <int ID> DI void ld2(u32x2& r, i32x4 rs, unsigned voff, unsigned soff) {
  asm volatile("buffer_load_dwordx2 %0, %1, %2, %3 offen" CNT : "=v"(r) : "v"(voff), "s"(rs), "s"(soff), [id] "i"(ID) : "memory");
}
template <int ID> DI void ldh(unsigned& r, i32x4 rs, unsigned voff) {
  asm volatile("buffer_load_ushort %0, %1, %2, 0 offen" CNT : "=v"(r) : "v"(voff), "s"(rs), [id] "i"(ID) : "memory");
}
template <int ID, int OFF> DI void st4_sc1(u32x4 v, i32x4 rs, unsigned voff, unsigned soff) {
  asm volatile("buffer_store_dwordx4 %0, %1, %2, %3 offen offset:%c4 sc1\n\ts_nop 1" CNT :: "v"(v), "v"(voff), "s"(rs), "s"(soff), "i"(OFF), [id] "i"(ID) : "memory");
}
template <int ID> DI void st1_sc1(unsigned v, i32x4 rs, unsigned voff, unsigned soff) {
  asm volatile("buffer_store_dword %0, %1, %2, %3 offen sc1" CNT :: "v"(v), "v"(voff), "s"(rs), "s"(soff), [id] "i"(ID) : "memory");
}
#define WAITS ".if (vmc-vmk%c[id]) < 63\n\ts_waitcnt vmcnt(vmc-vmk%c[id])\n\t.endif"
template <int ID> DI void need() { asm volatile(WAITS :: [id] "i"(ID) : "memory"); }
template <int ID, class T> DI void need(T& a) { asm volatile(WAITS : "+v"(a) : [id] "i"(ID) : "memory"); }
template <int ID, class T> DI void need(T& a, T& b) { asm volatile(WAITS : "+v"(a), "+v"(b) : [id] "i"(ID) : "memory"); }
DI unsigned opq(unsigned v) { asm volatile("" : "+v"(v)); return v; }
template <class T> DI void pin(T& v) { asm volatile("" : "+v"(v)); }
DI void fence() { __builtin_amdgcn_sched_barrier(0); }
DI void bar_nolds() { fence(); asm volatile("s_barrier" ::: "memory"); fence(); }
DI void bar() { fence(); asm volatile("s_waitcnt lgkmcnt(0)\n\ts_barrier" ::: "memory"); fence(); }
DI u32x4 lds128(unsigned a) { return *(LDSP u32x4*)a; }
DI f32x4 lds128f(unsigned a) { return *(LDSP f32x4*)a; }
DI f32x2 lds64f(unsigned a) { return *(LDSP f32x2*)a; }
DI u32x4 ldstr(unsigned a0, unsigned a1) {
  s16x4 lo = __builtin_amdgcn_ds_read_tr16_b64_v4i16((LDSP s16x4*)a0);
  s16x4 hi = __builtin_amdgcn_ds_read_tr16_b64_v4i16((LDSP s16x4*)a1);
  return __builtin_bit_cast(u32x4, __builtin_shufflevector(lo, hi, 0, 1, 2, 3, 4, 5, 6, 7));
}
DI f32x16 mfma(u32x4 a, u32x4 b, f32x16 c) {
  return __builtin_amdgcn_mfma_f32_32x32x16_bf16(__builtin_bit_cast(bf16x8, a), __builtin_bit_cast(bf16x8, b), c, 0, 0, 0);
}
DI f32x4 mfma16(u32x4 a, u32x4 b, f32x4 c) {
  return __builtin_amdgcn_mfma_f32_16x16x32_bf16(__builtin_bit_cast(bf16x8, a), __builtin_bit_cast(bf16x8, b), c, 0, 0, 0);
}
DI unsigned pk(float lo, float hi) {
  f32x2 p = {lo, hi};
  return __builtin_bit_cast(unsigned, __builtin_convertvector(p, bf16x2));
}
DI float bf2f(unsigned v) { return __uint_as_float(v << 16); }
DI float hi2f(unsigned v) { return __uint_as_float(v & 0xffff0000u); }
DI float halfsum(float a, float b) {
  auto r = __builtin_amdgcn_permlane32_swap(__float_as_uint(a), __float_as_uint(b), false, false);
  return __uint_as_float(r[0]) + __uint_as_float(r[1]);
}
DI float sum4(f32x4 v) { return (v[0] + v[1]) + (v[2] + v[3]); }
DI unsigned split_hl(float v) { const unsigned p = pk(v, v); return pk(v, v - hi2f(p)); }
DI u32x4 split_op(float v) { const unsigned p = pk(v, v); const float l = v - hi2f(p); return u32x4{p, pk(l, l), 0u, 0u}; }
DI unsigned tau(unsigned r) { return 16u * ((r >> 2) & 1u) + (r & 3u) + 4u * (r >> 3); }
DI unsigned swap23(unsigned r) { return (r & 0x13u) | ((r & 4u) << 1) | ((r & 8u) >> 1); }
DI f32x2 cmul(f32x2 a, f32x2 b) { return f32x2{a[0] * b[0] - a[1] * b[1], a[0] * b[1] + a[1] * b[0]}; }
DI f32x2 cmad(f32x2 a, f32x2 b, f32x2 c) { return f32x2{a[0] * b[0] + (c[0] - a[1] * b[1]), a[0] * b[1] + (c[1] + a[1] * b[0])}; }
DI f32x2 csq(f32x2 a) { return f32x2{a[0] * a[0] - a[1] * a[1], 2.0f * a[0] * a[1]}; }
constexpr unsigned ONES = 0x3F803F80u;
template <int FROM, int TO> DI void rebase(unsigned& a) {
  constexpr int d = (int)(RB(TO) & ~0xffffu) - (int)(RB(FROM) & ~0xffffu);
  if constexpr (d != 0) asm volatile("v_add_u32 %0, %1, %0" : "+v"(a) : "i"(d));
}
constexpr unsigned RO(int s) { return RB(s) & 0xffffu; }

#define STAMP(i) do { if (i == 0 || i == PROBE) stamp[i] = (unsigned)__builtin_amdgcn_s_memrealtime(); } while (0)

template <int I, int N, class Fn> DI void static_for(Fn&& f) {
  if constexpr (I < N) { f(IC<I>{}); static_for<I + 1, N>(f); }
}

DI void poll_flags(unsigned v, bool sel, unsigned tag, i32x4 rs, unsigned voff, unsigned soff) {
  if (__all(!sel || v == tag)) return;
  for (int it = 0; it < SPIN_CAP; ++it) {
    __builtin_amdgcn_s_sleep(1);
    unsigned w;
    asm volatile("buffer_load_dword %0, %1, %2, %3 offen sc1\n\ts_waitcnt vmcnt(0)" : "=&v"(w) : "v"(voff), "s"(rs), "s"(soff) : "memory");
    if (__all(!sel || w == tag)) return;
  }
}

struct GluK { float k1, k2, iv; };
DI unsigned glu_pair(const f32x16& a0, const f32x16& a1, int e, const GluK& k) {
  float o[2];
#pragma unroll
  for (int t = 0; t < 2; ++t) {
    const float a = a0[e + t];
    const float z = a * (k.k1 + k.k2 * (a * a));
    const float s = __builtin_amdgcn_rcpf(__builtin_amdgcn_exp2f(z) * k.iv + k.iv);
    o[t] = (a * a1[e + t]) * s;
  }
  return pk(o[0], o[1]);
}

#define C_FLAGE 13
constexpr int DA = 8;
#define T2_G 6
#define T3_G 22
#define PRO 10
#define I_FLAGE 3
#define I_FLAGL 5
constexpr int sched_m(int n) {
  int m = PRO;
  for (int t = 0; t < 64; ++t) {
    const int c = t >> 2, i = t & 3, sg = m >> 1;
    const int cf = i >= 2 ? c - 1 : c - 2;
    const int freed = cf < 8 ? 2 * cf + 1 : 8 + cf;
    bool go = m < 64 && sg <= (freed < -1 ? -1 : freed) + 9;
    if (sg >= 16 && sg < 24 && i == 1) go = false;
    if (t == n) return go ? m : -1;
    if (go) ++m;
  }
  return m;
}
static_assert(sched_m(64) == 64, "ring prefetch incomplete");
constexpr int issued_before(int n) { int m = PRO; for (int t = 0; t < n; ++t) if (sched_m(t) >= 0) ++m; return m; }

struct Args { const u16 *x, *lre, *lim, *Bre, *Bim, *Cre, *Cim, *ldt, *Dsk, *wg, *wu, *wd; float* out; char* ws; };

template <bool SSM>
DI void body(const Args& p, const unsigned rt, const unsigned s, unsigned* stamp) {
  constexpr int Z = SSM ? 1000 : 0;
  asm volatile(".set vmc, 0");
  const unsigned lane = threadIdx.x & 63u;
  const unsigned wv = __builtin_amdgcn_readfirstlane(threadIdx.x >> 6);
  const unsigned j = s >> 1, r = lane & 31u, h = lane >> 5, cg = wv >> 2, rg = wv & 3u;

  const i32x4 rsX = mk_rsrc(p.x, SEQ * DM * 2), rsG = mk_rsrc(p.wg, DM * FF * 2), rsU = mk_rsrc(p.wu, DM * FF * 2);
  const i32x4 rsB = mk_rsrc(cg ? p.Bim : p.Bre, DM * DM * 2);
  const i32x4 rsWD = mk_rsrc(p.wd, FF * DM * 2), rsCr = mk_rsrc(p.Cre, DM * DM * 2), rsCi = mk_rsrc(p.Cim, DM * DM * 2);
  const i32x4 rsWS = mk_rsrc(p.ws, WS_END);
  const unsigned ldsw = wv * 1024u;
  const unsigned row = 8u * wv + (lane >> 3), cp = lane & 7u;
  const unsigned sw = (cp ^ ((row >> 1) & 7u)) * 16u;
  const unsigned hs = (((cp >> 2) ^ ((row >> 1) & 1u)) * 4u + (cp & 3u)) * 16u;
  const unsigned vX = ((row & 32u) + tau(row & 31u)) * 1024u + sw;
  const unsigned vB = swap23(row & 31u) * 1024u + sw;
  const unsigned vG = row * (FF * 2u) + hs;
  const unsigned pairB = SSM ? 16u + (j & 3u) + 3u * (j >> 2) : 8u + j;
  const bool hasB = !(SSM && (j & 3u) == 3u);
  const unsigned sX = rt * (128u * 1024u);
  const unsigned sWB = hasB ? pairB * 128u : SOOB;
  unsigned vWD, vC;

  const unsigned sJ = j * 65536u, sK = j * 128u;
  auto fillh = [&](auto MM) {
    constexpr int S = MM >> 1;
    constexpr unsigned T = MM & 1, L = RB(S) + T * 8192u;
    constexpr int ID = Z + ID_F + S;
    if constexpr (S < 24) {
      constexpr unsigned ch = S < 16 ? S >> 1 : S - 16;
      if constexpr (S < 16 && (S & 1) == 0) glds<ID, ch * 128u + T * 65536u, L>(rsX, vX, sX, ldsw);
      else if constexpr (S < 16 && SSM) glds<ID, ch * 128u + T * 32768u, L>(rsB, vB, sJ, ldsw);
      else glds<ID, ch * (64u * FF * 2u), L>(T ? rsU : rsG, vG, S < 16 ? sK : sWB, ldsw);
    } else {
      constexpr unsigned i = S - 24;
      if constexpr (i >= 8 && i < 16) glds<ID, (i - 8u) * 128u, L>(T ? rsCi : rsCr, vC, sJ, ldsw);
      else glds<ID, (i < 8 ? i : i - 8u) * (128u * DM * 2u) + T * 65536u, L>(rsWD, vWD, sK, ldsw);
    }
  };

  unsigned eR, eC, plr, pli, pdt;
  asm volatile("s_nop 4" ::: "memory");
  if constexpr (SSM) {
    const unsigned po = (64u * j + 32u * cg + swap23(r)) * 2u;
    ldh<Z + ID_PAR>(plr, mk_rsrc(p.lre, 1024), po); ldh<Z + ID_PAR>(pli, mk_rsrc(p.lim, 1024), po); ldh<Z + ID_PAR>(pdt, mk_rsrc(p.ldt, 1024), po);
  }
  {
    const unsigned l8 = lane < 8u ? lane : 0x00ffffffu;
    touch<Z + ID_T>(rsX, l8 * 128u, sX + (s * 8u + wv) * 1024u);
    if constexpr (SSM) touch<Z + ID_T>(rsB, l8 * 128u, sJ + (rt * 4u + rg) * 1024u);
    else touch<Z + ID_T>(cg ? rsU : rsG, lane < 8u ? lane * (FF * 2u) : OOB, sK + (rt * 32u + rg * 8u) * (FF * 2u));
    unsigned long long pc;
    unsigned span;
    asm volatile("s_getpc_b64 %0\n.Ls5beg%c2:\n\ts_mov_b32 %1, .Ls5end%c2-.Ls5beg%c2" : "=s"(pc), "=s"(span) : "i"(Z));
    touch<Z + ID_T>(mk_rsrc((const void*)(pc & ~127ull), span), lane < 2u ? lane * 16384u : OOB, (((blockIdx.x >> 6) * 4u + (rt & 3u)) * 8u + wv) * 128u);
  }
  static_for<0, PRO>(fillh);
  ld1_sc1<Z + ID_M>(eR, rsWS, 0u, WS_EROW + rt * 128u);
  if constexpr (SSM) ld1_sc1<Z + ID_M>(eC, rsWS, 0u, WS_ECOL + j * 128u);

  f32x2 lam, cf;
  if constexpr (SSM) {
    asm volatile(WAITS : "+v"(plr), "+v"(pli), "+v"(pdt) : [id] "i"(Z + ID_PAR) : "memory");
    const float dt = bf2f(pk(expf(bf2f(pdt)), 0.f) & 0xffffu);
    const float Lr = bf2f(plr), Li = bf2f(pli);
    const float e = expf(dt * Lr);
    float sn, cs;
    sincosf(dt * Li, &sn, &cs);
    lam = f32x2{e * cs, e * sn};
    const float nr = lam[0] - 1.0f, inv = 2.0f / (Lr * Lr + Li * Li);
    cf = f32x2{(nr * Lr + lam[1] * Li) * inv, (lam[1] * Lr - nr * Li) * inv};
    pin(lam); pin(cf);
  }

  const unsigned rot = rg ^ (2u * cg);
  unsigned xa[4], wa[4];
  auto ga_init = [&](int i, unsigned win, unsigned ln) {
    const unsigned q4 = (ln & 15u) >> 2;
    return win + 128u * (16u * (i ^ rot) + 8u * (ln >> 5) + q4) + 64u * (cg ^ (q4 >> 1)) + 32u * ((ln >> 4) & 1u) + 8u * (ln & 3u);
  };
#pragma unroll
  for (int i = 0; i < 4; ++i) {
    const unsigned t = 128u * r + 16u * ((2u * (i ^ rot) + h) ^ ((r >> 1) & 7u));
    xa[i] = t + rg * 4096u;
    wa[i] = SSM ? t + cg * 8192u : ga_init(i, 0u, lane);
  }
  u32x4 xf[32], w0[2], w1[2];
  auto reads = [&](auto NN, auto MM) {
    constexpr int N = NN, M = MM, C = N >> 2, i = N & 3;
    constexpr int SW = C < 8 ? 2 * C + 1 : 8 + C;
    constexpr int SP = C == 0 ? SW : (C <= 8 ? 2 * C - 1 : 7 + C);
    if constexpr (M == 0) {
      if constexpr (C < 8) { rebase<(C == 0 ? 0 : 2 * C - 2), 2 * C>(xa[i]); xf[N] = lds128(xa[i] + RO(2 * C)); }
      if constexpr (SSM && C == 8) wa[i] = ga_init(i, RB(SW) & ~0xffffu, opq(lane)); else rebase<SP, SW>(wa[i]);
      if constexpr (SSM && C < 8) w0[N & 1] = lds128(wa[i] + RO(SW)); else w0[N & 1] = ldstr(wa[i] + RO(SW), wa[i] + RO(SW) + 512u);
    } else {
      if constexpr (SSM && C < 8) w1[N & 1] = lds128(wa[i] + RO(SW) + 4096u); else w1[N & 1] = ldstr(wa[i] + RO(SW) + 8192u, wa[i] + RO(SW) + 8704u);
    }
  };

  float sx0 = 0.f, sx1 = 0.f, sq0 = 0.f, sq1 = 0.f, mean = 0.f;
  f32x4 cs4 = {};
  u32x4 sel0, sel1;
  auto mksel = [&](unsigned ln, unsigned row, u32x4& sel) {
    const unsigned t = (ln & 15u) == row + ((ln >> 2) & 4u) ? ONES : 0u;
    sel = u32x4{t, t, t, t}; pin(sel);
  };
  mksel(lane, 0u, sel0); mksel(lane, 8u, sel1);
  GluK gk;
  unsigned opM = 0u, tagR = 0u, tagC = 0u, poll;
  f32x16 a0 = {}, a1 = {}, b0 = {}, b1 = {};
  f32x4 st4, ca4, cb4, gt, gp;
  f32x2 e1 = {0.f, 0.f}, pre = {0.f, 0.f}, l16, wseg, wj, l1k, p2, p4, sg[8];
  u32x4 o0, o1, g0, g1, opA, opB;
  const unsigned hrow = (rt * 4u + rg) * NQ;
  const unsigned gso = WS_AGG + (2u * j + cg) * 8192u;
  const unsigned fso = WS_CTL + rt * 256u;
  const unsigned lane0 = lane;
  const f32x2 one = {1.f, 0.f};

  need<Z + ID_F + 1>();
  bar();
  STAMP(1);
  reads(IC<0>{}, IC<0>{}); reads(IC<0>{}, IC<1>{}); reads(IC<1>{}, IC<0>{}); reads(IC<1>{}, IC<1>{});
  static_for<0, 128>([&](auto GG) {
    constexpr int G = GG, N = G >> 1, M = G & 1, C = N >> 2, i = N & 3;
    fence();
    {
      const u32x4 x = xf[N & 31], w = M ? w1[N & 1] : w0[N & 1];
      f32x16& acc = C >= 8 ? (M ? b1 : b0) : (M ? a1 : a0);
      if constexpr (SSM && C < 8) acc = mfma(x, w, acc); else acc = mfma(w, x, acc);
      if constexpr (i == (C == 8 ? 3 : 0)) cs4 = mfma16(M ? sel1 : sel0, w, cs4);
      if constexpr (G == 63 || G == 127) {
        const u32x4 m = {opM, opM, 0u, 0u};
        if constexpr (G == 127) { b0 = mfma(opA, m, b0); b1 = mfma(opB, m, b1); }
        else if constexpr (SSM) { a0 = mfma(m, opA, a0); a1 = mfma(m, opB, a1); }
        else { a0 = mfma(opA, m, a0); a1 = mfma(opB, m, a1); }
      }
    }
    fence();
    constexpr bool USE_LANE = (i == 2 && M == 0 && (C == 7 || C >= 9)) || G == 62 || (G >= 64 && G <= 92);
    const unsigned lane = USE_LANE ? opq(lane0) : lane0, r = lane & 31u, h = lane >> 5, j1 = 2u * rg + h, j2 = j1 + 8u;
    if constexpr (i == 2 && M == 0) {
      const bool leader = wv == 0u && lane == 0u;
      if constexpr (C == 7) *(LDSP float*)(L_STP + (rg * 32u + r) * 16u + (cg * 2u + h) * 4u) = halfsum(sx0 + sx1, sq0 + sq1);
      if constexpr (C == 7 || C == 15) { *(LDSP float*)(L_CSP + ((cg * 2u + h) * 32u + r) * 16u + rg * 4u) = cs4[0]; cs4 = f32x4{}; }
      if constexpr (SSM && C == 9) *(LDSP f32x2*)(L_SEG + ((cg * 8u + j1) * 32u + r) * 8u) = e1;
      if constexpr (SSM && C == 15) {
        need<Z + ID_G>(g0, g1);
        bool ok = (j1 >= rt || (g0[1] == tagC && g0[3] == tagC)) && (j2 >= rt || (g1[1] == tagC && g1[3] == tagC));
        if (!__all(ok)) {
          const unsigned gv1 = j1 < rt ? (j1 * 32u + r) * 16u : OOB, gv2 = j2 < rt ? (j2 * 32u + r) * 16u : OOB;
          for (int it = 0; it < SPIN_CAP; ++it) {
            __builtin_amdgcn_s_sleep(1);
            asm volatile("buffer_load_dwordx4 %0, %2, %4, %5 offen sc1\n\tbuffer_load_dwordx4 %1, %3, %4, %5 offen sc1\n\ts_waitcnt vmcnt(0)"
                         : "=&v"(g0), "=&v"(g1) : "v"(gv1), "v"(gv2), "s"(rsWS), "s"(gso) : "memory");
            ok = (j1 >= rt || (g0[1] == tagC && g0[3] == tagC)) && (j2 >= rt || (g1[1] == tagC && g1[3] == tagC));
            if (__all(ok)) break;
          }
        }
        const f32x2 t = cmul(wj, cmad(l1k, f32x2{__uint_as_float(g0[0]), __uint_as_float(g0[2])}, f32x2{__uint_as_float(g1[0]), __uint_as_float(g1[2])}));
        *(LDSP float*)(L_CAR + ((cg * 32u + r) * 4u + rg) * 8u + h * 4u) = halfsum(t[0], t[1]);
      }
      static_assert(issued_before(N) >= 2 * (C < 7 ? 2 * C + 3 : 9 + C) + 2, "slot not issued yet");
      need<Z + ID_F + (C < 7 ? 2 * C + 3 : 9 + C)>();
      if constexpr (!SSM && C == C_FLAGE) need<Z + ID_STE>();
      if constexpr (C == 6) { if constexpr (SSM) need<Z + ID_M>(eR, eC); else need<Z + ID_M>(eR); tagR = eR + 1u; if constexpr (SSM) tagC = eC + 1u; }
      if constexpr (C == 7 || C == 15 || (SSM && C == 9)) bar(); else bar_nolds();
      if constexpr (C == 7) st4 = lds128f(L_STP + (rg * 32u + r) * 16u);
      if constexpr (C == 7 || C == 15) { ca4 = lds128f(L_CSP + (cg * 64u + r) * 16u); cb4 = lds128f(L_CSP + (cg * 64u + 32u + r) * 16u); }
      if constexpr (SSM && C == 9) {
#pragma unroll
        for (int t = 0; t < 8; ++t) sg[t] = lds64f(L_SEG + ((cg * 8u + t) * 32u + r) * 8u);
      }
      if constexpr (SSM && C == 15) { g0 = lds128(L_CAR + (cg * 32u + r) * 32u); g1 = lds128(L_CAR + (cg * 32u + r) * 32u + 16u); }
      if constexpr (C == 9) {
        const unsigned row = 8u * wv + (lane >> 3), cp = lane & 7u;
        vWD = row * 1024u + (((cp >> 2) ^ ((row >> 1) & 1u)) * 4u + (cp & 3u)) * 16u;
      }
      if constexpr (!SSM && C == C_FLAGE) st1_sc1<Z + ID_M>(tagR, rsWS, leader ? s * 4u : OOB, fso);
      if constexpr (SSM && C == 14) {
        ld4_sc1<Z + ID_G, 0>(g0, rsWS, j1 < rt ? (j1 * 32u + r) * 16u : OOB, gso);
        ld4_sc1<Z + ID_G, 0>(g1, rsWS, j2 < rt ? (j2 * 32u + r) * 16u : OOB, gso);
      }
      if constexpr (C == 15) ld1_sc1<Z + ID_P1>(poll, rsWS, r * 4u, fso);
      if constexpr (SSM && C == 15) st1_sc1<Z + ID_M>(tagC, rsWS, (rt == 15u && leader) ? j * 128u : OOB, WS_ECOL);
      if constexpr (C == 7) STAMP(2);
    }
    if constexpr (N + 2 < 64) reads(IC<(N + 2 < 64 ? N + 2 : 0)>{}, IC<M>{});
    if constexpr (M == 1 && sched_m(N) >= 0) { static_assert(sched_m(N) < 48 || N >= 38, "vWD"); fillh(IC<(sched_m(N) >= 0 ? sched_m(N) : 0)>{}); }
    if constexpr (G == T2_G) touch<Z + ID_T>(cg ? rsU : rsG, lane < 8u ? lane * (FF * 2u) : OOB, sWB + (rt * 32u + rg * 8u) * (FF * 2u));
    if constexpr (G == T3_G) {
      const unsigned w = (rt * 2u + (s & 1u)) * 8u + wv;
      touch<Z + ID_T>(rsWD, lane < 6u ? lane * (256u * 1024u) : OOB, sK + w * 1024u);
      touch<Z + ID_T>((w & 1u) ? rsCi : rsCr, lane < 4u ? lane * 128u : OOB, sJ + (w >> 1) * 512u);
    }
    if constexpr (G < 60) {
      constexpr int D0 = C < 7 ? G & 7 : 2 * (G & 7), D1 = C < 7 ? D0 + 1 : D0 + 2;
#pragma unroll
      for (int d = D0; d < D1; ++d) {
        const unsigned v = xf[4 * C + (d >> 2)][d & 3];
        const float lo = bf2f(v), hi = hi2f(v);
        sx0 += lo; sx1 += hi; sq0 = fmaf(lo, lo, sq0); sq1 = fmaf(hi, hi, sq1);
      }
      pin(sx0); pin(sx1); pin(sq0); pin(sq1);
    }
    if constexpr (G == 69) mksel(lane, 0u, sel0);
    if constexpr (G == 70) mksel(lane, 8u, sel1);
    if constexpr (G == 61) {
      mean = (st4[0] + st4[2]) * (1.0f / DM);
      gk.iv = fmaxf((st4[1] + st4[3]) * (1.0f / DM) - mean * mean, 0.0f) + 1e-5f;
      pin(mean); pin(gk.iv);
    }
    if constexpr (G == 61 || G == 125) { opA = split_op(sum4(ca4)); pin(opA); }
    if constexpr (G == 62 || G == 126) { opB = split_op(sum4(cb4)); pin(opB); }
    if constexpr (G == 62) { opM = h ? 0u : split_hl(-mean); pin(opM); }
    if constexpr (G == 64) {
      const float ra = __builtin_amdgcn_rsqf(gk.iv);
      *(LDSP float*)(L_RA + h * 512u + (rg * 32u + tau(r)) * 4u) = h ? -mean * ra : ra;
      asm volatile("" ::: "memory");
      gk.k1 = -2.302208198f * ra;
      gk.k2 = -0.1029432397f * (ra * ra * ra);
      pin(gk.k1); pin(gk.k2);
    }
    if constexpr (!SSM && G >= 66 && G < 82) {
      constexpr int E = (G - 66) & ~3, ST = (G - 66) & 3;
#pragma unroll
      for (int t = 0; t < 4; ++t) {
        const float a = a0[E + t];
        if constexpr (ST == 0) gt[t] = gk.k1 + gk.k2 * (a * a);
        if constexpr (ST == 1) gt[t] = __builtin_amdgcn_exp2f(a * gt[t]);
        if constexpr (ST == 2) gt[t] = __builtin_amdgcn_rcpf(gt[t] * gk.iv + gk.iv);
        if constexpr (ST == 3) gt[t] *= a * a1[E + t];
      }
      if constexpr (ST == 3) {
        u32x4& o = E < 8 ? o0 : o1;
        o[(E & 4) / 2] = pk(gt[0], gt[1]); o[(E & 4) / 2 + 1] = pk(gt[2], gt[3]);
        if constexpr (E == 4) st4_sc1<Z + ID_STE, 0>(o0, rsWS, lane * 16u, WS_H + (hrow + 2u * j + cg) * 2048u);
        else if constexpr (E == 12) st4_sc1<Z + ID_STE, 1024>(o1, rsWS, lane * 16u, WS_H + (hrow + 2u * j + cg) * 2048u);
        else pin(o);
      } else pin(gt);
    }
    if constexpr (SSM) {
      if constexpr (G >= 67 && G < 75) {
        constexpr int T = 2 * (G - 67);
        const f32x4 rs = (T & 4) ? gp : st4;
#pragma unroll
        for (int t = T; t < T + 2; ++t) {
          a0[t] *= rs[t & 3]; a1[t] *= rs[t & 3];
          e1 = cmad(lam, e1, f32x2{a0[t], a1[t]});
        }
        pin(e1);
      }
      if constexpr (G == 65 || G == 68) st4 = lds128f(L_RA + (rg * 32u + 16u * h + (G == 65 ? 0u : 8u)) * 4u);
      if constexpr (G == 65 || G == 70) gp = lds128f(L_RA + (rg * 32u + 16u * h + (G == 65 ? 4u : 12u)) * 4u);
      if constexpr (G == 65) { l16 = csq(csq(lam)); pin(l16); }
      if constexpr (G == 66) { l16 = csq(csq(l16)); pin(l16); }
      if constexpr (G == 77) e1 = f32x2{0.f, 0.f};
      if constexpr (G >= 77 && G < 81) {
#pragma unroll
        for (int t = 2 * (G - 77); t < 2 * (G - 77) + 2; ++t) { if (t) pre = (unsigned)t == j1 ? e1 : pre; e1 = cmad(l16, e1, sg[t]); }
        pin(pre); pin(e1);
        if constexpr (G == 80) {
          st4_sc1<Z + ID_M, 0>(u32x4{__float_as_uint(e1[0]), tagC, __float_as_uint(e1[1]), tagC}, rsWS, (rg == 0u && h == 0u) ? (rt * 32u + r) * 16u : OOB, gso);
          STAMP(3);
        }
      }
      if constexpr (G == 82) { p2 = csq(l16); p4 = csq(p2); l1k = csq(p4); pin(p2); pin(p4); pin(l1k); }
      if constexpr (G == 83) { wseg = cmul((j1 & 1u) ? l16 : one, (j1 & 2u) ? p2 : one); pin(wseg); }
      if constexpr (G == 85) { wseg = cmul(wseg, (j1 & 4u) ? p4 : one); pin(wseg); }
      if constexpr (G == 86) { p2 = csq(l1k); p4 = csq(p2); pin(p2); pin(p4); }
      if constexpr (G == 87 || G == 88) {
        const unsigned e = j1 < rt ? rt - 1u - (j2 < rt ? j2 : j1) : 0u;
        if constexpr (G == 87) wj = cmul((e & 1u) ? l1k : one, (e & 2u) ? p2 : one);
        else wj = cmul(wj, (e & 4u) ? p4 : one);
        pin(wj);
      }
      if constexpr (G == 89) { l1k = j2 < rt ? csq(p4) : one; pin(l1k); }
    }
  });
  fence();
  STAMP(4);

  {
  const unsigned lane = opq(lane0), r = lane & 31u, h = lane >> 5, q4 = (lane & 15u) >> 2, avo = lane * 16u, fvo = r * 4u;
  const unsigned tid = wv * 64u + lane;
  const bool leader = wv == 0u && lane == 0u;
  const unsigned kg = rg, rh2 = cg, rh = s & 1u;
  const unsigned aso = WS_H + ((rt * 4u + rh * 2u + rh2) * NQ + kg) * 2048u;
  u32x4 A[19][2];
  auto lda = [&](auto II) { constexpr int I = II; ld4_sc1<Z + ID_A + I, 0>(A[I][0], rsWS, avo, aso + I * 8192u); ld4_sc1<Z + ID_A + I, 1024>(A[I][1], rsWS, avo, aso + I * 8192u); };
  need<Z + ID_P1>(poll);
  poll_flags(poll, r < 16u && (r & 1u), tagR, rsWS, fvo, fso);
  static_for<0, 4>(lda);
  fence();
  STAMP(5);
  if constexpr (SSM) {
    const f32x2 cin = {(__uint_as_float(g0[0]) + __uint_as_float(g0[2])) + (__uint_as_float(g1[0]) + __uint_as_float(g1[2])),
                       (__uint_as_float(g0[1]) + __uint_as_float(g0[3])) + (__uint_as_float(g1[1]) + __uint_as_float(g1[3]))};
    f32x2 e = cmad(wseg, cin, pre);
    unsigned pr_[8], pi_[8];
#pragma unroll
    for (int t = 0; t < 8; ++t) {
      e = cmad(lam, e, f32x2{a0[2 * t], a1[2 * t]});
      const f32x2 y0 = cmul(cf, e);
      e = cmad(lam, e, f32x2{a0[2 * t + 1], a1[2 * t + 1]});
      const f32x2 y1 = cmul(cf, e);
      pr_[t] = pk(y0[0], y1[0]); pi_[t] = pk(-y0[1], -y1[1]);
    }
    u32x4 id[2];
#pragma unroll
    for (int ks = 0; ks < 2; ++ks)
#pragma unroll
      for (int q = 0; q < 4; ++q) {
        const int jj = 8 * ks + 2 * q;
        const unsigned r0 = (jj & 3) + 8 * (jj >> 2) + 4u * h;
        id[ks][q] = r == r0 ? 0x3F80u : (r == r0 + 1u ? 0x3F800000u : 0u);
      }
    f32x16 zr = mfma(u32x4{pr_[0], pr_[1], pr_[2], pr_[3]}, id[0], f32x16{});
    f32x16 zi = mfma(u32x4{pi_[0], pi_[1], pi_[2], pi_[3]}, id[0], f32x16{});
    zr = mfma(u32x4{pr_[4], pr_[5], pr_[6], pr_[7]}, id[1], zr);
    zi = mfma(u32x4{pi_[4], pi_[5], pi_[6], pi_[7]}, id[1], zi);
    u32x4 o[4];
#pragma unroll
    for (int q = 0; q < 4; ++q) {
      o[0][q] = pk(zr[2 * q], zr[2 * q + 1]); o[1][q] = pk(zr[8 + 2 * q], zr[9 + 2 * q]);
      o[2][q] = pk(zi[2 * q], zi[2 * q + 1]); o[3][q] = pk(zi[8 + 2 * q], zi[9 + 2 * q]);
    }
    const unsigned so = WS_H + (hrow + 32u + 4u * j + cg) * 2048u;
    st4_sc1<Z + ID_STE, 0>(o[0], rsWS, avo, so); st4_sc1<Z + ID_STE, 1024>(o[1], rsWS, avo, so);
    st4_sc1<Z + ID_STE, 0>(o[2], rsWS, avo, so + 4096u); st4_sc1<Z + ID_STE, 1024>(o[3], rsWS, avo, so + 4096u);
    fence();
  }
  STAMP(6);
  {
#pragma unroll
    for (int e = 0; e < 4; ++e) o0[e] = glu_pair(b0, b1, 2 * e, gk);
#pragma unroll
    for (int e = 0; e < 4; ++e) o1[e] = glu_pair(b0, b1, 8 + 2 * e, gk);
    const unsigned un = 2u * pairB + cg;
    const unsigned so = (hasB && un < 43u) ? WS_H + (hrow + un + (SSM ? 32u : 0u)) * 2048u : SOOB;
    st4_sc1<Z + ID_STL, 0>(o0, rsWS, avo, so); st4_sc1<Z + ID_STL, 1024>(o1, rsWS, avo, so);
  }
  fence();
  STAMP(7);
  unsigned pa[2], ca[2];
#pragma unroll
  for (unsigned t = 0; t < 2; ++t) {
    pa[t] = (RB(24) & ~0xffffu) + 128u * (32u * kg + 4u * h + q4) + 64u * (t ^ (q4 >> 1)) + 32u * ((lane >> 4) & 1u) + 8u * (lane & 3u);
    ca[t] = (RB(32) & ~0xffffu) + (kg >> 1) * 8192u + 128u * r + 16u * ((4u * (kg & 1u) + 2u * t + h) ^ ((r >> 1) & 7u));
  }
  u32x4 bw[19][4];
  auto readsB = [&](auto II) {
    constexpr int I = II, S = 24 + I;
    if constexpr (I >= 8 && I < 16) {
      if constexpr (I > 8) { rebase<S - 1, S>(ca[0]); rebase<S - 1, S>(ca[1]); }
#pragma unroll
      for (int ks = 0; ks < 2; ++ks) { bw[I][2 * ks] = lds128(ca[ks] + RO(S)); bw[I][2 * ks + 1] = lds128(ca[ks] + RO(S) + 4096u); }
    } else {
      if constexpr (I > 0) { rebase<(I == 16 ? 31 : S - 1), S>(pa[0]); rebase<(I == 16 ? 31 : S - 1), S>(pa[1]); }
#pragma unroll
      for (int ks = 0; ks < 2; ++ks) {
        bw[I][2 * ks] = ldstr(pa[0] + RO(S) + 2048u * ks, pa[0] + RO(S) + 2048u * ks + 1024u);
        bw[I][2 * ks + 1] = ldstr(pa[1] + RO(S) + 2048u * ks, pa[1] + RO(S) + 2048u * ks + 1024u);
      }
    }
  };
  auto mm = [&](auto II) {
    constexpr int I = II;
    need<Z + ID_A + I>(A[I][0], A[I][1]);
    fence();
    if (I < 18 || kg != 3u) {
      a0 = mfma(bw[I][0], A[I][0], a0); a1 = mfma(bw[I][1], A[I][0], a1);
      a0 = mfma(bw[I][2], A[I][1], a0); a1 = mfma(bw[I][3], A[I][1], a1);
    }
    fence();
  };
  a0 = f32x16{}; a1 = f32x16{};
  u32x2 xv[2], dv;
  const unsigned orow = rh * 64u + tau(tid >> 4), ocol = j * 64u + (tid & 15u) * 4u;
  {
    const unsigned row = 8u * wv + (lane >> 3), cp = lane & 7u;
    vC = row * 1024u + (cp ^ ((row >> 1) & 7u)) * 16u;
  }
  readsB(IC<0>{});
  static_for<0, 10>([&](auto II) {
    constexpr int I = II;
    fence();
    if constexpr (I < 9) need<Z + ID_F + 25 + I>();
    if constexpr (SSM && I == I_FLAGE) need<Z + ID_STE>();
    if constexpr (I == I_FLAGL) need<Z + ID_STL>();
    bar();
    if constexpr (SSM && I == I_FLAGE) st1_sc1<Z + ID_M>(tagR, rsWS, leader ? s * 4u : OOB, fso);
    if constexpr (I == I_FLAGL) { st1_sc1<Z + ID_M>(tagR, rsWS, leader ? s * 4u : OOB, fso + 64u); STAMP(8); }
    if constexpr (I < 9) readsB(IC<(I < 9 ? I + 1 : 0)>{});
    fence();
    if constexpr (I == 0) { fillh(IC<64>{}); fillh(IC<65>{}); }
    fillh(IC<2 * (33 + I)>{}); fillh(IC<2 * (33 + I) + 1>{});
    if constexpr (I < 4) mm(II);
  });
  ld2<Z + ID_X>(xv[0], rsX, orow * 1024u + ocol * 2u, sX); ld2<Z + ID_X>(xv[1], rsX, orow * 1024u + ocol * 2u, sX + 32768u);
  ld2<Z + ID_X>(dv, mk_rsrc(p.Dsk, 1024), ocol * 2u, 0u);
  ld1_sc1<Z + ID_P2>(poll, rsWS, fvo, fso);
  need<Z + ID_P2>(poll);
  bar();
  poll_flags(poll, ((r >> 4) ^ r ^ 1u) & 1u, tagR, rsWS, fvo, fso);
  STAMP(9);
  ld1_sc1<Z + ID_P3>(poll, rsWS, fvo, fso);
  static_for<4, 4 + DA>(lda);
  static_for<4, 19>([&](auto II) {
    constexpr int I = II;
    if constexpr (I + DA == 16) {
      need<Z + ID_P3>(poll);
      poll_flags(poll, r >= 16u, tagR, rsWS, fvo, fso);
      st1_sc1<Z + ID_M>(tagR, rsWS, (s == 0u && leader) ? rt * 128u : OOB, WS_EROW);
    }
    if constexpr (I + 2 >= 10 && I + 2 <= 18) readsB(IC<(I + 2 >= 10 && I + 2 <= 18 ? I + 2 : 0)>{});
    mm(II);
    if constexpr (I + DA <= 18) lda(IC<(I + DA <= 18 ? I + DA : 0)>{});
  });
  STAMP(10);
  bar();
#pragma unroll
  for (int g = 0; g < 4; ++g) {
    const unsigned a = ((kg * 64u + rh2 * 32u + r) * 68u + 8u * g + 4u * h) * 4u;
    *(LDSP f32x4*)a = f32x4{a0[4 * g], a0[4 * g + 1], a0[4 * g + 2], a0[4 * g + 3]};
    *(LDSP f32x4*)(a + 128u) = f32x4{a1[4 * g], a1[4 * g + 1], a1[4 * g + 2], a1[4 * g + 3]};
  }
  fence();
  asm volatile(WAITS "\n.Ls5end%c[z]:" : "+v"(xv[0]), "+v"(xv[1]), "+v"(dv) : [id] "i"(Z + ID_X), [z] "i"(Z) : "memory");
  bar();
#pragma unroll
  for (unsigned m = 0; m < 2; ++m) {
    const unsigned a = ((m * 32u + (tid >> 4)) * 68u + (tid & 15u) * 4u) * 4u;
    const f32x4 pt = (lds128f(a) + lds128f(a + 17408u)) + (lds128f(a + 34816u) + lds128f(a + 52224u));
    const f32x2 ab = {*(LDSP float*)(L_RA + (orow + 32u * m) * 4u), *(LDSP float*)(L_RA + 512u + (orow + 32u * m) * 4u)};
    f32x4 o;
#pragma unroll
    for (int e = 0; e < 4; ++e) {
      const float xe = (e & 1) ? hi2f(xv[m][e >> 1]) : bf2f(xv[m][e >> 1]);
      const float de = (e & 1) ? hi2f(dv[e >> 1]) : bf2f(dv[e >> 1]);
      o[e] = pt[e] + xe + (xe * ab[0] + ab[1]) * de;
    }
    *(f32x4*)(p.out + (size_t)(rt * 128u + orow + 32u * m) * DM + ocol) = o;
  }
  }
  STAMP(11);
  if (wv == 0u && SSM == PROBE_SSM) {
    unsigned d = stamp[PROBE] - stamp[0]; d = d > 10000u ? 10000u : d;
    const unsigned tend = stamp[0] + 5000u + 8u * d;
    while ((int)((unsigned)__builtin_amdgcn_s_memrealtime() - tend) < 0) __builtin_amdgcn_s_sleep(2);
  }
}

#if defined(__HIP_DEVICE_COMPILE__)
__attribute__((target("no-packed-fp32-ops")))
#endif
__global__ __launch_bounds__(512, 2) void s5_geglu_kernel(const Args p) {
  __shared__ __attribute__((aligned(16))) char smem[LDS_BYTES];
  unsigned stamp[32] = {};
  STAMP(0);
  asm volatile("" :: "v"((unsigned)(uintptr_t)(LDSP char*)smem));
  const unsigned b = blockIdx.x, xcd = b & 7u, idx = b >> 3;
  const unsigned rt = (xcd >> 1) * 4u + (idx & 3u), s = (xcd & 1u) * 8u + (idx >> 2);
  if ((s & 1u) == 0u) body<true>(p, rt, s, stamp);
  else body<false>(p, rt, s, stamp);
}

extern "C" void kernel_launch(void* const* d_in, const int* in_sizes, int n_in, void* d_out, int out_size, void* d_ws,
                              size_t ws_size, hipStream_t stream) {
  Args a;
  a.x = (const u16*)d_in[0]; a.lre = (const u16*)d_in[1]; a.lim = (const u16*)d_in[2]; a.Bre = (const u16*)d_in[3];
  a.Bim = (const u16*)d_in[4]; a.Cre = (const u16*)d_in[5]; a.Cim = (const u16*)d_in[6]; a.ldt = (const u16*)d_in[7];
  a.Dsk = (const u16*)d_in[8]; a.wg = (const u16*)d_in[10]; a.wu = (const u16*)d_in[11]; a.wd = (const u16*)d_in[12];
  a.out = (float*)d_out; a.ws = (char*)d_ws;
  s5_geglu_kernel<<<256, 512, 0, stream>>>(a);
}
```

```cpp
#define PROBE 0
#define PROBE_SSM false
#define OUT_AUX 17
#include <hip/hip_runtime.h>
#include <stdint.h>
#include <type_traits>

typedef unsigned short u16;
typedef __attribute__((ext_vector_type(4))) short s16x4;
typedef __attribute__((ext_vector_type(8))) __bf16 bf16x8;
typedef __attribute__((ext_vector_type(2))) __bf16 bf16x2;
typedef __attribute__((ext_vector_type(4))) unsigned u32x4;
typedef __attribute__((ext_vector_type(2))) unsigned u32x2;
typedef __attribute__((ext_vector_type(4))) int i32x4;
typedef __attribute__((ext_vector_type(2))) float f32x2;
typedef __attribute__((ext_vector_type(4))) float f32x4;
typedef __attribute__((ext_vector_type(16))) float f32x16;
#define LDSP __attribute__((address_space(3)))
#define DI __device__ __forceinline__
template <int V> using IC = std::integral_constant<int, V>;

constexpr unsigned SEQ = 2048, DM = 512, FF = 1376;
constexpr unsigned NQ = 75;
constexpr unsigned WS_H = 0;
constexpr unsigned WS_AGG = 64u * NQ * 2048u;
constexpr unsigned WS_CTL = WS_AGG + 16u * 16u * 512u;
constexpr unsigned WS_EROW = WS_CTL + 4096u;
constexpr unsigned WS_ECOL = WS_CTL + 8192u;
constexpr unsigned WS_END = WS_CTL + 12288u;
constexpr unsigned OOB = 0x7ffffff0u;
constexpr unsigned SOOB = 0x40000000u;

constexpr unsigned SCR = 9u * 16384u;
constexpr unsigned L_STP = SCR;
constexpr unsigned L_CAR = SCR + 2048u;
constexpr unsigned L_SEG = SCR + 4096u;
constexpr unsigned L_CSP = SCR + 8192u;
constexpr unsigned L_RA = SCR + 10240u;
constexpr unsigned L_TCH = SCR + 11264u;
constexpr unsigned LDS_BYTES = 163840u;
constexpr unsigned RB(int s) { return (unsigned)(s % 9) * 16384u; }

#define SPIN_CAP 20000
enum { ID_F = 100, ID_A = 200, ID_STE = 300, ID_STL = 301, ID_G = 310, ID_P1 = 320, ID_P2 = 321, ID_P3 = 322, ID_PAR = 330, ID_X = 340, ID_T = 350, ID_M = 399 };
#define CNT "\n\t.set vmc, vmc+1\n\t.set vmk%c[id], vmc"

DI i32x4 mk_rsrc(const void* p, unsigned bytes) {
  unsigned long long a = (unsigned long long)p;
  i32x4 r;
  r[0] = (int)(unsigned)a; r[1] = (int)((unsigned)(a >> 32) & 0xffffu); r[2] = (int)bytes; r[3] = 0x00020000;
  return r;
}
template <int ID, unsigned SOFF, unsigned LOFF> DI void glds(i32x4 rs, unsigned voff, unsigned sbase, unsigned ldsw) {
  unsigned t;
  asm volatile("s_add_u32 m0, %4, %6\n\ts_add_u32 %0, %3, %5\n\tbuffer_load_dwordx4 %1, %2, %0 offen lds" CNT
               : "=&s"(t) : "v"(voff), "s"(rs), "s"(sbase), "s"(ldsw), "i"(SOFF), "i"(LOFF), [id] "i"(ID) : "memory", "scc");
}
template <int ID> DI void touch(i32x4 rs, unsigned voff, unsigned soff) {
  asm volatile("s_mov_b32 m0, %3\n\ts_nop 0\n\tbuffer_load_dword %0, %1, %2 offen lds" CNT
               :: "v"(voff), "s"(rs), "s"(soff), "i"(L_TCH), [id] "i"(ID) : "memory");
}
template <int ID, int OFF> DI void ld4_sc1(u32x4& r, i32x4 rs, unsigned voff, unsigned soff) {
  asm volatile("buffer_load_dwordx4 %0, %1, %2, %3 offen offset:%c4 sc1" CNT : "=v"(r) : "v"(voff), "s"(rs), "s"(soff), "i"(OFF), [id] "i"(ID) : "memory");
}
template <int ID> DI void ld1_sc1(unsigned& r, i32x4 rs, unsigned voff, unsigned soff) {
  asm volatile("buffer_load_dword %0, %1, %2, %3 offen sc1" CNT : "=v"(r) : "v"(voff), "s"(rs), "s"(soff), [id] "i"(ID) : "memory");
}
template <int ID> DI void ld2(u32x2& r, i32x4 rs, unsigned voff, unsigned soff) {
  asm volatile("buffer_load_dwordx2 %0, %1, %2, %3 offen" CNT : "=v"(r) : "v"(voff), "s"(rs), "s"(soff), [id] "i"(ID) : "memory");
}
template <int ID> DI void ldh(unsigned& r, i32x4 rs, unsigned voff) {
  asm volatile("buffer_load_ushort %0, %1, %2, 0 offen" CNT : "=v"(r) : "v"(voff), "s"(rs), [id] "i"(ID) : "memory");
}
template <int ID, int OFF> DI void st4_sc1(u32x4 v, i32x4 rs, unsigned voff, unsigned soff) {
  asm volatile("buffer_store_dwordx4 %0, %1, %2, %3 offen offset:%c4 sc1\n\ts_nop 1" CNT :: "v"(v), "v"(voff), "s"(rs), "s"(soff), "i"(OFF), [id] "i"(ID) : "memory");
}
template <int ID> DI void st1_sc1(unsigned v, i32x4 rs, unsigned voff, unsigned soff) {
  asm volatile("buffer_store_dword %0, %1, %2, %3 offen sc1" CNT :: "v"(v), "v"(voff), "s"(rs), "s"(soff), [id] "i"(ID) : "memory");
}
#define WAITS ".if (vmc-vmk%c[id]) < 63\n\ts_waitcnt vmcnt(vmc-vmk%c[id])\n\t.endif"
template <int ID> DI void need() { asm volatile(WAITS :: [id] "i"(ID) : "memory"); }
template <int ID, class T> DI void need(T& a) { asm volatile(WAITS : "+v"(a) : [id] "i"(ID) : "memory"); }
template <int ID, class T> DI void need(T& a, T& b) { asm volatile(WAITS : "+v"(a), "+v"(b) : [id] "i"(ID) : "memory"); }
DI unsigned opq(unsigned v) { asm volatile("" : "+v"(v)); return v; }
template <class T> DI void pin(T& v) { asm volatile("" : "+v"(v)); }
DI void fence() { __builtin_amdgcn_sched_barrier(0); }
DI void bar_nolds() { fence(); asm volatile("s_barrier" ::: "memory"); fence(); }
DI void bar() { fence(); asm volatile("s_waitcnt lgkmcnt(0)\n\ts_barrier" ::: "memory"); fence(); }
DI u32x4 lds128(unsigned a) { return *(LDSP u32x4*)a; }
DI f32x4 lds128f(unsigned a) { return *(LDSP f32x4*)a; }
DI f32x2 lds64f(unsigned a) { return *(LDSP f32x2*)a; }
DI u32x4 ldstr(unsigned a0, unsigned a1) {
  s16x4 lo = __builtin_amdgcn_ds_read_tr16_b64_v4i16((LDSP s16x4*)a0);
  s16x4 hi = __builtin_amdgcn_ds_read_tr16_b64_v4i16((LDSP s16x4*)a1);
  return __builtin_bit_cast(u32x4, __builtin_shufflevector(lo, hi, 0, 1, 2, 3, 4, 5, 6, 7));
}
DI f32x16 mfma(u32x4 a, u32x4 b, f32x16 c) {
  return __builtin_amdgcn_mfma_f32_32x32x16_bf16(__builtin_bit_cast(bf16x8, a), __builtin_bit_cast(bf16x8, b), c, 0, 0, 0);
}
DI f32x4 mfma16(u32x4 a, u32x4 b, f32x4 c) {
  return __builtin_amdgcn_mfma_f32_16x16x32_bf16(__builtin_bit_cast(bf16x8, a), __builtin_bit_cast(bf16x8, b), c, 0, 0, 0);
}
DI unsigned pk(float lo, float hi) {
  f32x2 p = {lo, hi};
  return __builtin_bit_cast(unsigned, __builtin_convertvector(p, bf16x2));
}
DI float bf2f(unsigned v) { return __uint_as_float(v << 16); }
DI float hi2f(unsigned v) { return __uint_as_float(v & 0xffff0000u); }
DI float halfsum(float a, float b) {
  auto r = __builtin_amdgcn_permlane32_swap(__float_as_uint(a), __float_as_uint(b), false, false);
  return __uint_as_float(r[0]) + __uint_as_float(r[1]);
}
DI float sum4(f32x4 v) { return (v[0] + v[1]) + (v[2] + v[3]); }
DI unsigned split_hl(float v) { const unsigned p = pk(v, v); return pk(v, v - hi2f(p)); }
DI u32x4 split_op(float v) { const unsigned p = pk(v, v); const float l = v - hi2f(p); return u32x4{p, pk(l, l), 0u, 0u}; }
DI unsigned tau(unsigned r) { return 16u * ((r >> 2) & 1u) + (r & 3u) + 4u * (r >> 3); }
DI unsigned swap23(unsigned r) { return (r & 0x13u) | ((r & 4u) << 1) | ((r & 8u) >> 1); }
DI f32x2 cmul(f32x2 a, f32x2 b) { return f32x2{a[0] * b[0] - a[1] * b[1], a[0] * b[1] + a[1] * b[0]}; }
DI f32x2 cmad(f32x2 a, f32x2 b, f32x2 c) { return f32x2{a[0] * b[0] + (c[0] - a[1] * b[1]), a[0] * b[1] + (c[1] + a[1] * b[0])}; }
DI f32x2 csq(f32x2 a) { return f32x2{a[0] * a[0] - a[1] * a[1], 2.0f * a[0] * a[1]}; }
constexpr unsigned ONES = 0x3F803F80u;
template <int FROM, int TO> DI void rebase(unsigned& a) {
  constexpr int d = (int)(RB(TO) & ~0xffffu) - (int)(RB(FROM) & ~0xffffu);
  if constexpr (d != 0) asm volatile("v_add_u32 %0, %1, %0" : "+v"(a) : "i"(d));
}
constexpr unsigned RO(int s) { return RB(s) & 0xffffu; }

#define STAMP(i) do { if (i == 0 || i == PROBE) stamp[i] = (unsigned)__builtin_amdgcn_s_memrealtime(); } while (0)

template <int I, int N, class Fn> DI void static_for(Fn&& f) {
  if constexpr (I < N) { f(IC<I>{}); static_for<I + 1, N>(f); }
}

DI void poll_flags(unsigned v, bool sel, unsigned tag, i32x4 rs, unsigned voff, unsigned soff) {
  if (__all(!sel || v == tag)) return;
  for (int it = 0; it < SPIN_CAP; ++it) {
    __builtin_amdgcn_s_sleep(1);
    unsigned w;
    asm volatile("buffer_load_dword %0, %1, %2, %3 offen sc1\n\ts_waitcnt vmcnt(0)" : "=&v"(w) : "v"(voff), "s"(rs), "s"(soff) : "memory");
    if (__all(!sel || w == tag)) return;
  }
}

struct GluK { float k1, k2, iv; };
DI unsigned glu_pair(const f32x16& a0, const f32x16& a1, int e, const GluK& k) {
  float o[2];
#pragma unroll
  for (int t = 0; t < 2; ++t) {
    const float a = a0[e + t];
    const float z = a * (k.k1 + k.k2 * (a * a));
    const float s = __builtin_amdgcn_rcpf(__builtin_amdgcn_exp2f(z) * k.iv + k.iv);
    o[t] = (a * a1[e + t]) * s;
  }
  return pk(o[0], o[1]);
}

#define C_FLAGE 13
constexpr int DA = 8;
#define T2_G 6
#define T3_G 22
#define PRO 10
#define I_FLAGE 3
#define I_FLAGL 5
constexpr int sched_m(int n) {
  int m = PRO;
  for (int t = 0; t < 64; ++t) {
    const int c = t >> 2, i = t & 3, sg = m >> 1;
    const int cf = i >= 2 ? c - 1 : c - 2;
    const int freed = cf < 8 ? 2 * cf + 1 : 8 + cf;
    bool go = m < 64 && sg <= (freed < -1 ? -1 : freed) + 9;
    if (sg >= 16 && sg < 24 && i == 1) go = false;
    if (t == n) return go ? m : -1;
    if (go) ++m;
  }
  return m;
}
static_assert(sched_m(64) == 64, "ring prefetch incomplete");
constexpr int issued_before(int n) { int m = PRO; for (int t = 0; t < n; ++t) if (sched_m(t) >= 0) ++m; return m; }

struct Args { const u16 *x, *lre, *lim, *Bre, *Bim, *Cre, *Cim, *ldt, *Dsk, *wg, *wu, *wd; float* out; char* ws; };

template <bool SSM>
DI void body(const Args& p, const unsigned rt, const unsigned s, unsigned* stamp) {
  constexpr int Z = SSM ? 1000 : 0;
  asm volatile(".set vmc, 0");
  const unsigned lane = threadIdx.x & 63u;
  const unsigned wv = __builtin_amdgcn_readfirstlane(threadIdx.x >> 6);
  const unsigned j = s >> 1, r = lane & 31u, h = lane >> 5, cg = wv >> 2, rg = wv & 3u;

  const i32x4 rsX = mk_rsrc(p.x, SEQ * DM * 2), rsG = mk_rsrc(p.wg, DM * FF * 2), rsU = mk_rsrc(p.wu, DM * FF * 2);
  const i32x4 rsB = mk_rsrc(cg ? p.Bim : p.Bre, DM * DM * 2);
  const i32x4 rsWD = mk_rsrc(p.wd, FF * DM * 2), rsCr = mk_rsrc(p.Cre, DM * DM * 2), rsCi = mk_rsrc(p.Cim, DM * DM * 2);
  const i32x4 rsWS = mk_rsrc(p.ws, WS_END);
  const unsigned ldsw = wv * 1024u;
  const unsigned row = 8u * wv + (lane >> 3), cp = lane & 7u;
  const unsigned sw = (cp ^ ((row >> 1) & 7u)) * 16u;
  const unsigned hs = (((cp >> 2) ^ ((row >> 1) & 1u)) * 4u + (cp & 3u)) * 16u;
  const unsigned vX = ((row & 32u) + tau(row & 31u)) * 1024u + sw;
  const unsigned vB = swap23(row & 31u) * 1024u + sw;
  const unsigned vG = row * (FF * 2u) + hs;
  const unsigned pairB = SSM ? 16u + (j & 3u) + 3u * (j >> 2) : 8u + j;
  const bool hasB = !(SSM && (j & 3u) == 3u);
  const unsigned sX = rt * (128u * 1024u);
  const unsigned sWB = hasB ? pairB * 128u : SOOB;
  unsigned vWD, vC;

  const unsigned sJ = j * 65536u, sK = j * 128u;
  auto fillh = [&](auto MM) {
    constexpr int S = MM >> 1;
    constexpr unsigned T = MM & 1, L = RB(S) + T * 8192u;
    constexpr int ID = Z + ID_F + S;
    if constexpr (S < 24) {
      constexpr unsigned ch = S < 16 ? S >> 1 : S - 16;
      if constexpr (S < 16 && (S & 1) == 0) glds<ID, ch * 128u + T * 65536u, L>(rsX, vX, sX, ldsw);
      else if constexpr (S < 16 && SSM) glds<ID, ch * 128u + T * 32768u, L>(rsB, vB, sJ, ldsw);
      else glds<ID, ch * (64u * FF * 2u), L>(T ? rsU : rsG, vG, S < 16 ? sK : sWB, ldsw);
    } else {
      constexpr unsigned i = S - 24;
      if constexpr (i >= 8 && i < 16) glds<ID, (i - 8u) * 128u, L>(T ? rsCi : rsCr, vC, sJ, ldsw);
      else glds<ID, (i < 8 ? i : i - 8u) * (128u * DM * 2u) + T * 65536u, L>(rsWD, vWD, sK, ldsw);
    }
  };

  unsigned eR, eC, plr, pli, pdt;
  asm volatile("s_nop 4" ::: "memory");
  if constexpr (SSM) {
    const unsigned po = (64u * j + 32u * cg + swap23(r)) * 2u;
    ldh<Z + ID_PAR>(plr, mk_rsrc(p.lre, 1024), po); ldh<Z + ID_PAR>(pli, mk_rsrc(p.lim, 1024), po); ldh<Z + ID_PAR>(pdt, mk_rsrc(p.ldt, 1024), po);
  }
  {
    const unsigned l8 = lane < 8u ? lane : 0x00ffffffu;
    touch<Z + ID_T>(rsX, l8 * 128u, sX + (s * 8u + wv) * 1024u);
    if constexpr (SSM) touch<Z + ID_T>(rsB, l8 * 128u, sJ + (rt * 4u + rg) * 1024u);
    else touch<Z + ID_T>(cg ? rsU : rsG, lane < 8u ? lane * (FF * 2u) : OOB, sK + (rt * 32u + rg * 8u) * (FF * 2u));
    unsigned long long pc;
    unsigned span;
    asm volatile("s_getpc_b64 %0\n.Ls5beg%c2:\n\ts_mov_b32 %1, .Ls5end%c2-.Ls5beg%c2" : "=s"(pc), "=s"(span) : "i"(Z));
    touch<Z + ID_T>(mk_rsrc((const void*)(pc & ~127ull), span), lane < 2u ? lane * 16384u : OOB, (((blockIdx.x >> 6) * 4u + (rt & 3u)) * 8u + wv) * 128u);
  }
  static_for<0, PRO>(fillh);
  ld1_sc1<Z + ID_M>(eR, rsWS, 0u, WS_EROW + rt * 128u);
  if constexpr (SSM) ld1_sc1<Z + ID_M>(eC, rsWS, 0u, WS_ECOL + j * 128u);

  f32x2 lam, cf;
  if constexpr (SSM) {
    asm volatile(WAITS : "+v"(plr), "+v"(pli), "+v"(pdt) : [id] "i"(Z + ID_PAR) : "memory");
    const float dt = bf2f(pk(expf(bf2f(pdt)), 0.f) & 0xffffu);
    const float Lr = bf2f(plr), Li = bf2f(pli);
    const float e = expf(dt * Lr);
    float sn, cs;
    sincosf(dt * Li, &sn, &cs);
    lam = f32x2{e * cs, e * sn};
    const float nr = lam[0] - 1.0f, inv = 2.0f / (Lr * Lr + Li * Li);
    cf = f32x2{(nr * Lr + lam[1] * Li) * inv, (lam[1] * Lr - nr * Li) * inv};
    pin(lam); pin(cf);
  }

  const unsigned rot = rg ^ (2u * cg);
  unsigned xa[4], wa[4];
  auto ga_init = [&](int i, unsigned win, unsigned ln) {
    const unsigned q4 = (ln & 15u) >> 2;
    return win + 128u * (16u * (i ^ rot) + 8u * (ln >> 5) + q4) + 64u * (cg ^ (q4 >> 1)) + 32u * ((ln >> 4) & 1u) + 8u * (ln & 3u);
  };
#pragma unroll
  for (int i = 0; i < 4; ++i) {
    const unsigned t = 128u * r + 16u * ((2u * (i ^ rot) + h) ^ ((r >> 1) & 7u));
    xa[i] = t + rg * 4096u;
    wa[i] = SSM ? t + cg * 8192u : ga_init(i, 0u, lane);
  }
  u32x4 xf[32], w0[2], w1[2];
  auto reads = [&](auto NN, auto MM) {
    constexpr int N = NN, M = MM, C = N >> 2, i = N & 3;
    constexpr int SW = C < 8 ? 2 * C + 1 : 8 + C;
    constexpr int SP = C == 0 ? SW : (C <= 8 ? 2 * C - 1 : 7 + C);
    if constexpr (M == 0) {
      if constexpr (C < 8) { rebase<(C == 0 ? 0 : 2 * C - 2), 2 * C>(xa[i]); xf[N] = lds128(xa[i] + RO(2 * C)); }
      if constexpr (SSM && C == 8) wa[i] = ga_init(i, RB(SW) & ~0xffffu, opq(lane)); else rebase<SP, SW>(wa[i]);
      if constexpr (SSM && C < 8) w0[N & 1] = lds128(wa[i] + RO(SW)); else w0[N & 1] = ldstr(wa[i] + RO(SW), wa[i] + RO(SW) + 512u);
    } else {
      if constexpr (SSM && C < 8) w1[N & 1] = lds128(wa[i] + RO(SW) + 4096u); else w1[N & 1] = ldstr(wa[i] + RO(SW) + 8192u, wa[i] + RO(SW) + 8704u);
    }
  };

  float sx0 = 0.f, sx1 = 0.f, sq0 = 0.f, sq1 = 0.f, mean = 0.f;
  f32x4 cs4 = {};
  u32x4 sel0, sel1;
  auto mksel = [&](unsigned ln, unsigned row, u32x4& sel) {
    const unsigned t = (ln & 15u) == row + ((ln >> 2) & 4u) ? ONES : 0u;
    sel = u32x4{t, t, t, t}; pin(sel);
  };
  mksel(lane, 0u, sel0); mksel(lane, 8u, sel1);
  GluK gk;
  unsigned opM = 0u, tagR = 0u, tagC = 0u, poll;
  f32x16 a0 = {}, a1 = {}, b0 = {}, b1 = {};
  f32x4 st4, ca4, cb4, gt, gp;
  f32x2 e1 = {0.f, 0.f}, pre = {0.f, 0.f}, l16, wseg, wj, l1k, p2, p4, sg[8];
  u32x4 o0, o1, g0, g1, opA, opB;
  const unsigned hrow = (rt * 4u + rg) * NQ;
  const unsigned gso = WS_AGG + (2u * j + cg) * 8192u;
  const unsigned fso = WS_CTL + rt * 256u;
  const unsigned lane0 = lane;
  const f32x2 one = {1.f, 0.f};

  need<Z + ID_F + 1>();
  bar();
  STAMP(1);
  reads(IC<0>{}, IC<0>{}); reads(IC<0>{}, IC<1>{}); reads(IC<1>{}, IC<0>{}); reads(IC<1>{}, IC<1>{});
  static_for<0, 128>([&](auto GG) {
    constexpr int G = GG, N = G >> 1, M = G & 1, C = N >> 2, i = N & 3;
    fence();
    {
      const u32x4 x = xf[N & 31], w = M ? w1[N & 1] : w0[N & 1];
      f32x16& acc = C >= 8 ? (M ? b1 : b0) : (M ? a1 : a0);
      if constexpr (SSM && C < 8) acc = mfma(x, w, acc); else acc = mfma(w, x, acc);
      if constexpr (i == (C == 8 ? 3 : 0)) cs4 = mfma16(M ? sel1 : sel0, w, cs4);
      if constexpr (G == 63 || G == 127) {
        const u32x4 m = {opM, opM, 0u, 0u};
        if constexpr (G == 127) { b0 = mfma(opA, m, b0); b1 = mfma(opB, m, b1); }
        else if constexpr (SSM) { a0 = mfma(m, opA, a0); a1 = mfma(m, opB, a1); }
        else { a0 = mfma(opA, m, a0); a1 = mfma(opB, m, a1); }
      }
    }
    fence();
    constexpr bool USE_LANE = (i == 2 && M == 0 && (C == 7 || C >= 9)) || G == 62 || (G >= 64 && G <= 92);
    const unsigned lane = USE_LANE ? opq(lane0) : lane0, r = lane & 31u, h = lane >> 5, j1 = 2u * rg + h, j2 = j1 + 8u;
    if constexpr (i == 2 && M == 0) {
      const bool leader = wv == 0u && lane == 0u;
      if constexpr (C == 7) *(LDSP float*)(L_STP + (rg * 32u + r) * 16u + (cg * 2u + h) * 4u) = halfsum(sx0 + sx1, sq0 + sq1);
      if constexpr (C == 7 || C == 15) { *(LDSP float*)(L_CSP + ((cg * 2u + h) * 32u + r) * 16u + rg * 4u) = cs4[0]; cs4 = f32x4{}; }
      if constexpr (SSM && C == 9) *(LDSP f32x2*)(L_SEG + ((cg * 8u + j1) * 32u + r) * 8u) = e1;
      if constexpr (SSM && C == 15) {
        need<Z + ID_G>(g0, g1);
        bool ok = (j1 >= rt || (g0[1] == tagC && g0[3] == tagC)) && (j2 >= rt || (g1[1] == tagC && g1[3] == tagC));
        if (!__all(ok)) {
          const unsigned gv1 = j1 < rt ? (j1 * 32u + r) * 16u : OOB, gv2 = j2 < rt ? (j2 * 32u + r) * 16u : OOB;
          for (int it = 0; it < SPIN_CAP; ++it) {
            __builtin_amdgcn_s_sleep(1);
            asm volatile("buffer_load_dwordx4 %0, %2, %4, %5 offen sc1\n\tbuffer_load_dwordx4 %1, %3, %4, %5 offen sc1\n\ts_waitcnt vmcnt(0)"
                         : "=&v"(g0), "=&v"(g1) : "v"(gv1), "v"(gv2), "s"(rsWS), "s"(gso) : "memory");
            ok = (j1 >= rt || (g0[1] == tagC && g0[3] == tagC)) && (j2 >= rt || (g1[1] == tagC && g1[3] == tagC));
            if (__all(ok)) break;
          }
        }
        const f32x2 t = cmul(wj, cmad(l1k, f32x2{__uint_as_float(g0[0]), __uint_as_float(g0[2])}, f32x2{__uint_as_float(g1[0]), __uint_as_float(g1[2])}));
        *(LDSP float*)(L_CAR + ((cg * 32u + r) * 4u + rg) * 8u + h * 4u) = halfsum(t[0], t[1]);
      }
      static_assert(issued_before(N) >= 2 * (C < 7 ? 2 * C + 3 : 9 + C) + 2, "slot not issued yet");
      need<Z + ID_F + (C < 7 ? 2 * C + 3 : 9 + C)>();
      if constexpr (!SSM && C == C_FLAGE) need<Z + ID_STE>();
      if constexpr (C == 6) { if constexpr (SSM) need<Z + ID_M>(eR, eC); else need<Z + ID_M>(eR); tagR = eR + 1u; if constexpr (SSM) tagC = eC + 1u; }
      if constexpr (C == 7 || C == 15 || (SSM && C == 9)) bar(); else bar_nolds();
      if constexpr (C == 7) st4 = lds128f(L_STP + (rg * 32u + r) * 16u);
      if constexpr (C == 7 || C == 15) { ca4 = lds128f(L_CSP + (cg * 64u + r) * 16u); cb4 = lds128f(L_CSP + (cg * 64u + 32u + r) * 16u); }
      if constexpr (SSM && C == 9) {
#pragma unroll
        for (int t = 0; t < 8; ++t) sg[t] = lds64f(L_SEG + ((cg * 8u + t) * 32u + r) * 8u);
      }
      if constexpr (SSM && C == 15) { g0 = lds128(L_CAR + (cg * 32u + r) * 32u); g1 = lds128(L_CAR + (cg * 32u + r) * 32u + 16u); }
      if constexpr (C == 9) {
        const unsigned row = 8u * wv + (lane >> 3), cp = lane & 7u;
        vWD = row * 1024u + (((cp >> 2) ^ ((row >> 1) & 1u)) * 4u + (cp & 3u)) * 16u;
      }
      if constexpr (!SSM && C == C_FLAGE) st1_sc1<Z + ID_M>(tagR, rsWS, leader ? s * 4u : OOB, fso);
      if constexpr (SSM && C == 14) {
        ld4_sc1<Z + ID_G, 0>(g0, rsWS, j1 < rt ? (j1 * 32u + r) * 16u : OOB, gso);
        ld4_sc1<Z + ID_G, 0>(g1, rsWS, j2 < rt ? (j2 * 32u + r) * 16u : OOB, gso);
      }
      if constexpr (C == 15) ld1_sc1<Z + ID_P1>(poll, rsWS, r * 4u, fso);
      if constexpr (SSM && C == 15) st1_sc1<Z + ID_M>(tagC, rsWS, (rt == 15u && leader) ? j * 128u : OOB, WS_ECOL);
      if constexpr (C == 7) STAMP(2);
    }
    if constexpr (N + 2 < 64) reads(IC<(N + 2 < 64 ? N + 2 : 0)>{}, IC<M>{});
    if constexpr (M == 1 && sched_m(N) >= 0) { static_assert(sched_m(N) < 48 || N >= 38, "vWD"); fillh(IC<(sched_m(N) >= 0 ? sched_m(N) : 0)>{}); }
    if constexpr (G == T2_G) touch<Z + ID_T>(cg ? rsU : rsG, lane < 8u ? lane * (FF * 2u) : OOB, sWB + (rt * 32u + rg * 8u) * (FF * 2u));
    if constexpr (G == T3_G) {
      const unsigned w = (rt * 2u + (s & 1u)) * 8u + wv;
      touch<Z + ID_T>(rsWD, lane < 6u ? lane * (256u * 1024u) : OOB, sK + w * 1024u);
      touch<Z + ID_T>((w & 1u) ? rsCi : rsCr, lane < 4u ? lane * 128u : OOB, sJ + (w >> 1) * 512u);
    }
    if constexpr (G < 60) {
      constexpr int D0 = C < 7 ? G & 7 : 2 * (G & 7), D1 = C < 7 ? D0 + 1 : D0 + 2;
#pragma unroll
      for (int d = D0; d < D1; ++d) {
        const unsigned v = xf[4 * C + (d >> 2)][d & 3];
        const float lo = bf2f(v), hi = hi2f(v);
        sx0 += lo; sx1 += hi; sq0 = fmaf(lo, lo, sq0); sq1 = fmaf(hi, hi, sq1);
      }
      pin(sx0); pin(sx1); pin(sq0); pin(sq1);
    }
    if constexpr (G == 69) mksel(lane, 0u, sel0);
    if constexpr (G == 70) mksel(lane, 8u, sel1);
    if constexpr (G == 61) {
      mean = (st4[0] + st4[2]) * (1.0f / DM);
      gk.iv = fmaxf((st4[1] + st4[3]) * (1.0f / DM) - mean * mean, 0.0f) + 1e-5f;
      pin(mean); pin(gk.iv);
    }
    if constexpr (G == 61 || G == 125) { opA = split_op(sum4(ca4)); pin(opA); }
    if constexpr (G == 62 || G == 126) { opB = split_op(sum4(cb4)); pin(opB); }
    if constexpr (G == 62) { opM = h ? 0u : split_hl(-mean); pin(opM); }
    if constexpr (G == 64) {
      const float ra = __builtin_amdgcn_rsqf(gk.iv);
      *(LDSP float*)(L_RA + h * 512u + (rg * 32u + tau(r)) * 4u) = h ? -mean * ra : ra;
      asm volatile("" ::: "memory");
      gk.k1 = -2.302208198f * ra;
      gk.k2 = -0.1029432397f * (ra * ra * ra);
      pin(gk.k1); pin(gk.k2);
    }
    if constexpr (!SSM && G >= 66 && G < 82) {
      constexpr int E = (G - 66) & ~3, ST = (G - 66) & 3;
#pragma unroll
      for (int t = 0; t < 4; ++t) {
        const float a = a0[E + t];
        if constexpr (ST == 0) gt[t] = gk.k1 + gk.k2 * (a * a);
        if constexpr (ST == 1) gt[t] = __builtin_amdgcn_exp2f(a * gt[t]);
        if constexpr (ST == 2) gt[t] = __builtin_amdgcn_rcpf(gt[t] * gk.iv + gk.iv);
        if constexpr (ST == 3) gt[t] *= a * a1[E + t];
      }
      if constexpr (ST == 3) {
        u32x4& o = E < 8 ? o0 : o1;
        o[(E & 4) / 2] = pk(gt[0], gt[1]); o[(E & 4) / 2 + 1] = pk(gt[2], gt[3]);
        if constexpr (E == 4) st4_sc1<Z + ID_STE, 0>(o0, rsWS, lane * 16u, WS_H + (hrow + 2u * j + cg) * 2048u);
        else if constexpr (E == 12) st4_sc1<Z + ID_STE, 1024>(o1, rsWS, lane * 16u, WS_H + (hrow + 2u * j + cg) * 2048u);
        else pin(o);
      } else pin(gt);
    }
    if constexpr (SSM) {
      if constexpr (G >= 67 && G < 75) {
        constexpr int T = 2 * (G - 67);
        const f32x4 rs = (T & 4) ? gp : st4;
#pragma unroll
        for (int t = T; t < T + 2; ++t) {
          a0[t] *= rs[t & 3]; a1[t] *= rs[t & 3];
          e1 = cmad(lam, e1, f32x2{a0[t], a1[t]});
        }
        pin(e1);
      }
      if constexpr (G == 65 || G == 68) st4 = lds128f(L_RA + (rg * 32u + 16u * h + (G == 65 ? 0u : 8u)) * 4u);
      if constexpr (G == 65 || G == 70) gp = lds128f(L_RA + (rg * 32u + 16u * h + (G == 65 ? 4u : 12u)) * 4u);
      if constexpr (G == 65) { l16 = csq(csq(lam)); pin(l16); }
      if constexpr (G == 66) { l16 = csq(csq(l16)); pin(l16); }
      if constexpr (G == 77) e1 = f32x2{0.f, 0.f};
      if constexpr (G >= 77 && G < 81) {
#pragma unroll
        for (int t = 2 * (G - 77); t < 2 * (G - 77) + 2; ++t) { if (t) pre = (unsigned)t == j1 ? e1 : pre; e1 = cmad(l16, e1, sg[t]); }
        pin(pre); pin(e1);
        if constexpr (G == 80) {
          st4_sc1<Z + ID_M, 0>(u32x4{__float_as_uint(e1[0]), tagC, __float_as_uint(e1[1]), tagC}, rsWS, (rg == 0u && h == 0u) ? (rt * 32u + r) * 16u : OOB, gso);
          STAMP(3);
        }
      }
      if constexpr (G == 82) { p2 = csq(l16); p4 = csq(p2); l1k = csq(p4); pin(p2); pin(p4); pin(l1k); }
      if constexpr (G == 83) { wseg = cmul((j1 & 1u) ? l16 : one, (j1 & 2u) ? p2 : one); pin(wseg); }
      if constexpr (G == 85) { wseg = cmul(wseg, (j1 & 4u) ? p4 : one); pin(wseg); }
      if constexpr (G == 86) { p2 = csq(l1k); p4 = csq(p2); pin(p2); pin(p4); }
      if constexpr (G == 87 || G == 88) {
        const unsigned e = j1 < rt ? rt - 1u - (j2 < rt ? j2 : j1) : 0u;
        if constexpr (G == 87) wj = cmul((e & 1u) ? l1k : one, (e & 2u) ? p2 : one);
        else wj = cmul(wj, (e & 4u) ? p4 : one);
        pin(wj);
      }
      if constexpr (G == 89) { l1k = j2 < rt ? csq(p4) : one; pin(l1k); }
    }
  });
  fence();
  STAMP(4);

  {
  const unsigned lane = opq(lane0), r = lane & 31u, h = lane >> 5, q4 = (lane & 15u) >> 2, avo = lane * 16u, fvo = r * 4u;
  const unsigned tid = wv * 64u + lane;
  const bool leader = wv == 0u && lane == 0u;
  const unsigned kg = rg, rh2 = cg, rh = s & 1u;
  const unsigned aso = WS_H + ((rt * 4u + rh * 2u + rh2) * NQ + kg) * 2048u;
  u32x4 A[19][2];
  auto lda = [&](auto II) { constexpr int I = II; ld4_sc1<Z + ID_A + I, 0>(A[I][0], rsWS, avo, aso + I * 8192u); ld4_sc1<Z + ID_A + I, 1024>(A[I][1], rsWS, avo, aso + I * 8192u); };
  need<Z + ID_P1>(poll);
  poll_flags(poll, r < 16u && (r & 1u), tagR, rsWS, fvo, fso);
  static_for<0, 4>(lda);
  fence();
  STAMP(5);
  if constexpr (SSM) {
    const f32x2 cin = {(__uint_as_float(g0[0]) + __uint_as_float(g0[2])) + (__uint_as_float(g1[0]) + __uint_as_float(g1[2])),
                       (__uint_as_float(g0[1]) + __uint_as_float(g0[3])) + (__uint_as_float(g1[1]) + __uint_as_float(g1[3]))};
    f32x2 e = cmad(wseg, cin, pre);
    unsigned pr_[8], pi_[8];
#pragma unroll
    for (int t = 0; t < 8; ++t) {
      e = cmad(lam, e, f32x2{a0[2 * t], a1[2 * t]});
      const f32x2 y0 = cmul(cf, e);
      e = cmad(lam, e, f32x2{a0[2 * t + 1], a1[2 * t + 1]});
      const f32x2 y1 = cmul(cf, e);
      pr_[t] = pk(y0[0], y1[0]); pi_[t] = pk(-y0[1], -y1[1]);
    }
    u32x4 id[2];
#pragma unroll
    for (int ks = 0; ks < 2; ++ks)
#pragma unroll
      for (int q = 0; q < 4; ++q) {
        const int jj = 8 * ks + 2 * q;
        const unsigned r0 = (jj & 3) + 8 * (jj >> 2) + 4u * h;
        id[ks][q] = r == r0 ? 0x3F80u : (r == r0 + 1u ? 0x3F800000u : 0u);
      }
    f32x16 zr = mfma(u32x4{pr_[0], pr_[1], pr_[2], pr_[3]}, id[0], f32x16{});
    f32x16 zi = mfma(u32x4{pi_[0], pi_[1], pi_[2], pi_[3]}, id[0], f32x16{});
    zr = mfma(u32x4{pr_[4], pr_[5], pr_[6], pr_[7]}, id[1], zr);
    zi = mfma(u32x4{pi_[4], pi_[5], pi_[6], pi_[7]}, id[1], zi);
    u32x4 o[4];
#pragma unroll
    for (int q = 0; q < 4; ++q) {
      o[0][q] = pk(zr[2 * q], zr[2 * q + 1]); o[1][q] = pk(zr[8 + 2 * q], zr[9 + 2 * q]);
      o[2][q] = pk(zi[2 * q], zi[2 * q + 1]); o[3][q] = pk(zi[8 + 2 * q], zi[9 + 2 * q]);
    }
    const unsigned so = WS_H + (hrow + 32u + 4u * j + cg) * 2048u;
    st4_sc1<Z + ID_STE, 0>(o[0], rsWS, avo, so); st4_sc1<Z + ID_STE, 1024>(o[1], rsWS, avo, so);
    st4_sc1<Z + ID_STE, 0>(o[2], rsWS, avo, so + 4096u); st4_sc1<Z + ID_STE, 1024>(o[3], rsWS, avo, so + 4096u);
    fence();
  }
  STAMP(6);
  {
#pragma unroll
    for (int e = 0; e < 4; ++e) o0[e] = glu_pair(b0, b1, 2 * e, gk);
#pragma unroll
    for (int e = 0; e < 4; ++e) o1[e] = glu_pair(b0, b1, 8 + 2 * e, gk);
    const unsigned un = 2u * pairB + cg;
    const unsigned so = (hasB && un < 43u) ? WS_H + (hrow + un + (SSM ? 32u : 0u)) * 2048u : SOOB;
    st4_sc1<Z + ID_STL, 0>(o0, rsWS, avo, so); st4_sc1<Z + ID_STL, 1024>(o1, rsWS, avo, so);
  }
  fence();
  STAMP(7);
  unsigned pa[2], ca[2];
#pragma unroll
  for (unsigned t = 0; t < 2; ++t) {
    pa[t] = (RB(24) & ~0xffffu) + 128u * (32u * kg + 4u * h + q4) + 64u * (t ^ (q4 >> 1)) + 32u * ((lane >> 4) & 1u) + 8u * (lane & 3u);
    ca[t] = (RB(32) & ~0xffffu) + (kg >> 1) * 8192u + 128u * r + 16u * ((4u * (kg & 1u) + 2u * t + h) ^ ((r >> 1) & 7u));
  }
  u32x4 bw[19][4];
  auto readsB = [&](auto II) {
    constexpr int I = II, S = 24 + I;
    if constexpr (I >= 8 && I < 16) {
      if constexpr (I > 8) { rebase<S - 1, S>(ca[0]); rebase<S - 1, S>(ca[1]); }
#pragma unroll
      for (int ks = 0; ks < 2; ++ks) { bw[I][2 * ks] = lds128(ca[ks] + RO(S)); bw[I][2 * ks + 1] = lds128(ca[ks] + RO(S) + 4096u); }
    } else {
      if constexpr (I > 0) { rebase<(I == 16 ? 31 : S - 1), S>(pa[0]); rebase<(I == 16 ? 31 : S - 1), S>(pa[1]); }
#pragma unroll
      for (int ks = 0; ks < 2; ++ks) {
        bw[I][2 * ks] = ldstr(pa[0] + RO(S) + 2048u * ks, pa[0] + RO(S) + 2048u * ks + 1024u);
        bw[I][2 * ks + 1] = ldstr(pa[1] + RO(S) + 2048u * ks, pa[1] + RO(S) + 2048u * ks + 1024u);
      }
    }
  };
  auto mm = [&](auto II) {
    constexpr int I = II;
    need<Z + ID_A + I>(A[I][0], A[I][1]);
    fence();
    if (I < 18 || kg != 3u) {
      a0 = mfma(bw[I][0], A[I][0], a0); a1 = mfma(bw[I][1], A[I][0], a1);
      a0 = mfma(bw[I][2], A[I][1], a0); a1 = mfma(bw[I][3], A[I][1], a1);
    }
    fence();
  };
  a0 = f32x16{}; a1 = f32x16{};
  u32x2 xv[2], dv;
  const unsigned orow = rh * 64u + tau(tid >> 4), ocol = j * 64u + (tid & 15u) * 4u;
  {
    const unsigned row = 8u * wv + (lane >> 3), cp = lane & 7u;
    vC = row * 1024u + (cp ^ ((row >> 1) & 7u)) * 16u;
  }
  readsB(IC<0>{});
  static_for<0, 10>([&](auto II) {
    constexpr int I = II;
    fence();
    if constexpr (I < 9) need<Z + ID_F + 25 + I>();
    if constexpr (SSM && I == I_FLAGE) need<Z + ID_STE>();
    if constexpr (I == I_FLAGL) need<Z + ID_STL>();
    bar();
    if constexpr (SSM && I == I_FLAGE) st1_sc1<Z + ID_M>(tagR, rsWS, leader ? s * 4u : OOB, fso);
    if constexpr (I == I_FLAGL) { st1_sc1<Z + ID_M>(tagR, rsWS, leader ? s * 4u : OOB, fso + 64u); STAMP(8); }
    if constexpr (I < 9) readsB(IC<(I < 9 ? I + 1 : 0)>{});
    fence();
    if constexpr (I == 0) { fillh(IC<64>{}); fillh(IC<65>{}); }
    fillh(IC<2 * (33 + I)>{}); fillh(IC<2 * (33 + I) + 1>{});
    if constexpr (I < 4) mm(II);
  });
  ld2<Z + ID_X>(xv[0], rsX, orow * 1024u + ocol * 2u, sX); ld2<Z + ID_X>(xv[1], rsX, orow * 1024u + ocol * 2u, sX + 32768u);
  ld2<Z + ID_X>(dv, mk_rsrc(p.Dsk, 1024), ocol * 2u, 0u);
  ld1_sc1<Z + ID_P2>(poll, rsWS, fvo, fso);
  need<Z + ID_P2>(poll);
  bar();
  poll_flags(poll, ((r >> 4) ^ r ^ 1u) & 1u, tagR, rsWS, fvo, fso);
  STAMP(9);
  ld1_sc1<Z + ID_P3>(poll, rsWS, fvo, fso);
  static_for<4, 4 + DA>(lda);
  static_for<4, 19>([&](auto II) {
    constexpr int I = II;
    if constexpr (I + DA == 16) {
      need<Z + ID_P3>(poll);
      poll_flags(poll, r >= 16u, tagR, rsWS, fvo, fso);
      st1_sc1<Z + ID_M>(tagR, rsWS, (s == 0u && leader) ? rt * 128u : OOB, WS_EROW);
    }
    if constexpr (I + 2 >= 10 && I + 2 <= 18) readsB(IC<(I + 2 >= 10 && I + 2 <= 18 ? I + 2 : 0)>{});
    mm(II);
    if constexpr (I + DA <= 18) lda(IC<(I + DA <= 18 ? I + DA : 0)>{});
  });
  STAMP(10);
  bar();
#pragma unroll
  for (int g = 0; g < 4; ++g) {
    const unsigned a = ((kg * 64u + rh2 * 32u + r) * 68u + 8u * g + 4u * h) * 4u;
    *(LDSP f32x4*)a = f32x4{a0[4 * g], a0[4 * g + 1], a0[4 * g + 2], a0[4 * g + 3]};
    *(LDSP f32x4*)(a + 128u) = f32x4{a1[4 * g], a1[4 * g + 1], a1[4 * g + 2], a1[4 * g + 3]};
  }
  fence();
  asm volatile(WAITS "\n.Ls5end%c[z]:" : "+v"(xv[0]), "+v"(xv[1]), "+v"(dv) : [id] "i"(Z + ID_X), [z] "i"(Z) : "memory");
  bar();
#pragma unroll
  for (unsigned m = 0; m < 2; ++m) {
    const unsigned a = ((m * 32u + (tid >> 4)) * 68u + (tid & 15u) * 4u) * 4u;
    const f32x4 pt = (lds128f(a) + lds128f(a + 17408u)) + (lds128f(a + 34816u) + lds128f(a + 52224u));
    const f32x2 ab = {*(LDSP float*)(L_RA + (orow + 32u * m) * 4u), *(LDSP float*)(L_RA + 512u + (orow + 32u * m) * 4u)};
    f32x4 o;
#pragma unroll
    for (int e = 0; e < 4; ++e) {
      const float xe = (e & 1) ? hi2f(xv[m][e >> 1]) : bf2f(xv[m][e >> 1]);
      const float de = (e & 1) ? hi2f(dv[e >> 1]) : bf2f(dv[e >> 1]);
      o[e] = pt[e] + xe + (xe * ab[0] + ab[1]) * de;
    }
    __builtin_amdgcn_raw_buffer_store_b128(__builtin_bit_cast(u32x4, o), __builtin_amdgcn_make_buffer_rsrc(p.out, 0, SEQ * DM * 4, 0x00020000),
                                           ((orow + 32u * m) * DM + ocol) * 4u, rt * (128u * DM * 4u), OUT_AUX);
  }
  }
  STAMP(11);
#define PROBE_COND (SSM == PROBE_SSM)
  if (wv == 0u && PROBE_COND) {
    unsigned d = stamp[PROBE] - stamp[0]; d = d > 10000u ? 10000u : d;
    const unsigned tend = stamp[0] + 5000u + 8u * d;
    while ((int)((unsigned)__builtin_amdgcn_s_memrealtime() - tend) < 0) __builtin_amdgcn_s_sleep(2);
  }
}

#if defined(__HIP_DEVICE_COMPILE__)
__attribute__((target("no-packed-fp32-ops")))
#endif
__global__ __launch_bounds__(512, 2) void s5_geglu_kernel(const Args p) {
  __shared__ __attribute__((aligned(16))) char smem[LDS_BYTES];
  unsigned stamp[32] = {};
  STAMP(0);
  asm volatile("" :: "v"((unsigned)(uintptr_t)(LDSP char*)smem));
  const unsigned b = blockIdx.x, xcd = b & 7u, idx = b >> 3;
  const unsigned rt = (xcd >> 1) * 4u + (idx & 3u), s = (xcd & 1u) * 8u + (idx >> 2);
  if ((s & 1u) == 0u) body<true>(p, rt, s, stamp);
  else body<false>(p, rt, s, stamp);
}

extern "C" void kernel_launch(void* const* d_in, const int* in_sizes, int n_in, void* d_out, int out_size, void* d_ws,
                              size_t ws_size, hipStream_t stream) {
  Args a;
  a.x = (const u16*)d_in[0]; a.lre = (const u16*)d_in[1]; a.lim = (const u16*)d_in[2]; a.Bre = (const u16*)d_in[3];
  a.Bim = (const u16*)d_in[4]; a.Cre = (const u16*)d_in[5]; a.Cim = (const u16*)d_in[6]; a.ldt = (const u16*)d_in[7];
  a.Dsk = (const u16*)d_in[8]; a.wg = (const u16*)d_in[10]; a.wu = (const u16*)d_in[11]; a.wd = (const u16*)d_in[12];
  a.out = (float*)d_out; a.ws = (char*)d_ws;
  s5_geglu_kernel<<<256, 512, 0, stream>>>(a);
}
```

```cpp
#include <hip/hip_runtime.h>
#include <stdint.h>
#include <type_traits>
#pragma clang diagnostic ignored "-Wint-to-pointer-cast"

typedef unsigned short u16;
typedef __attribute__((ext_vector_type(4))) short s16x4;
typedef __attribute__((ext_vector_type(8))) __bf16 bf16x8;
typedef __attribute__((ext_vector_type(2))) __bf16 bf16x2;
typedef __attribute__((ext_vector_type(4))) unsigned u32x4;
typedef __attribute__((ext_vector_type(2))) unsigned u32x2;
typedef __attribute__((ext_vector_type(4))) int i32x4;
typedef __attribute__((ext_vector_type(2))) float f32x2;
typedef __attribute__((ext_vector_type(4))) float f32x4;
typedef __attribute__((ext_vector_type(16))) float f32x16;
#define LDSP __attribute__((address_space(3)))
#define DI __device__ __forceinline__
template <int V> using IC = std::integral_constant<int, V>;

constexpr unsigned SEQ = 2048, DM = 512, FF = 1376;
constexpr unsigned NQ = 75;
constexpr unsigned WS_H = 0;
constexpr unsigned WS_AGG = 64u * NQ * 2048u;
constexpr unsigned WS_STAT = WS_AGG + 16u * 16u * 512u;
constexpr unsigned WS_CSB = WS_STAT + 32768u;
constexpr unsigned WS_MBOX = WS_CSB + 8192u;
constexpr unsigned WS_EPO = WS_MBOX + 32768u;
constexpr unsigned WS_END = WS_EPO + 128u;
constexpr unsigned OOB = 0x7ffffff0u;
constexpr unsigned SOOB = 0x40000000u;

constexpr unsigned L_CAR = 0u;
constexpr unsigned L_RA = 1024u;
constexpr unsigned L_CSP = 2048u;
constexpr unsigned L_SEG = 4096u;
constexpr unsigned L_TCH = 7936u;
constexpr unsigned L_ID = 8192u;
constexpr unsigned L_WJ = 10240u;
constexpr unsigned L_RED = 16384u;
constexpr unsigned LDS_BYTES = 163840u;
constexpr unsigned RB(int s) { return 16384u + (unsigned)s * 16384u; }
constexpr int SL(int c) { return c < 6 ? c : (c - 6) % 4; }
constexpr unsigned SB(int c) { return 16384u + (unsigned)SL(c) * 24576u; }
constexpr unsigned WB(int c) { return 16384u + (unsigned)(SL(c) & ~1) * 24576u; }
constexpr unsigned RO(int c) { return SB(c) - WB(c); }

constexpr int SPIN_CAP = 20000;
enum { ID_F = 100, ID_W = 150, ID_C = 170, ID_A = 200, ID_STE = 300, ID_STL = 301, ID_G = 310, ID_P2 = 321, ID_P3 = 322, ID_PAR = 330, ID_ROW = 331, ID_S = 332, ID_X = 340, ID_M = 399 };
#define CNT "\n\t.set vmc, vmc+1\n\t.set vmk%c[id], vmc"

DI i32x4 mk_rsrc(const void* p, unsigned bytes) {
  unsigned long long a = (unsigned long long)p;
  i32x4 r;
  r[0] = (int)(unsigned)a; r[1] = (int)((unsigned)(a >> 32) & 0xffffu); r[2] = (int)bytes; r[3] = 0x00020000;
  return r;
}
template <int ID, unsigned SOFF, unsigned LOFF> DI void glds(i32x4 rs, unsigned voff, unsigned sbase, unsigned ldsw) {
  unsigned t;
  asm volatile("s_add_u32 m0, %4, %6\n\ts_add_u32 %0, %3, %5\n\tbuffer_load_dwordx4 %1, %2, %0 offen lds" CNT
               : "=&s"(t) : "v"(voff), "s"(rs), "s"(sbase), "s"(ldsw), "i"(SOFF), "i"(LOFF), [id] "i"(ID) : "memory", "scc");
}
template <int ID, unsigned SX, unsigned LOFF> DI void glds_x(i32x4 rs, unsigned voff, unsigned sbase, unsigned ldsw) {
  unsigned t;
  asm volatile("s_add_u32 m0, %4, %6\n\ts_xor_b32 %0, %3, %5\n\tbuffer_load_dwordx4 %1, %2, %0 offen lds" CNT
               : "=&s"(t) : "v"(voff), "s"(rs), "s"(sbase), "s"(ldsw), "i"(SX), "i"(LOFF), [id] "i"(ID) : "memory", "scc");
}
template <int ID, unsigned CH, unsigned STRIDE, unsigned LOFF> DI void glds_m(i32x4 rs, unsigned voff, unsigned sbase, unsigned rk, unsigned ldsw) {
  unsigned t;
  asm volatile("s_add_u32 m0, %4, %6\n\ts_xor_b32 %0, %7, %5\n\ts_mul_i32 %0, %0, %8\n\ts_add_u32 %0, %0, %3\n\tbuffer_load_dwordx4 %1, %2, %0 offen lds" CNT
               : "=&s"(t) : "v"(voff), "s"(rs), "s"(sbase), "s"(ldsw), "i"(CH), "i"(LOFF), "s"(rk), "i"(STRIDE), [id] "i"(ID) : "memory", "scc");
}
#define IFON(body) "s_cmp_lg_u32 %[on], 0\n\ts_cbranch_scc1 1f\n\t" body "\n1:"
DI void touch_if(unsigned on, i32x4 rs, unsigned voff, unsigned soff) {
  asm volatile(IFON("s_mov_b32 m0, %3\n\ts_nop 0\n\tbuffer_load_dword %0, %1, %2 offen lds")
               :: "v"(voff), "s"(rs), "s"(soff), "i"(L_TCH), [on] "s"(on) : "memory", "scc");
}
DI void st1_if(unsigned on, unsigned v, i32x4 rs, unsigned voff, unsigned soff) {
  asm volatile(IFON("buffer_store_dword %0, %1, %2, %3 offen sc1") :: "v"(v), "v"(voff), "s"(rs), "s"(soff), [on] "s"(on) : "memory", "scc");
}
DI void st4_if(unsigned on, u32x4 v, i32x4 rs, unsigned voff, unsigned soff) {
  asm volatile(IFON("buffer_store_dwordx4 %0, %1, %2, %3 offen sc1\n\ts_nop 1") :: "v"(v), "v"(voff), "s"(rs), "s"(soff), [on] "s"(on) : "memory", "scc");
}
template <int ID, int OFF> DI void ld4_sc1(u32x4& r, i32x4 rs, unsigned voff, unsigned soff) {
  asm volatile("buffer_load_dwordx4 %0, %1, %2, %3 offen offset:%c4 sc1" CNT : "=v"(r) : "v"(voff), "s"(rs), "s"(soff), "i"(OFF), [id] "i"(ID) : "memory");
}
template <int ID> DI void ld4(u32x4& r, i32x4 rs, unsigned voff, unsigned soff) {
  asm volatile("buffer_load_dwordx4 %0, %1, %2, %3 offen" CNT : "=v"(r) : "v"(voff), "s"(rs), "s"(soff), [id] "i"(ID) : "memory");
}
template <int ID> DI void st2_sc1(u32x2 v, i32x4 rs, unsigned voff, unsigned soff) {
  asm volatile("buffer_store_dwordx2 %0, %1, %2, %3 offen sc1" CNT :: "v"(v), "v"(voff), "s"(rs), "s"(soff), [id] "i"(ID) : "memory");
}
template <int ID> DI void ld1_sc1(unsigned& r, i32x4 rs, unsigned voff, unsigned soff) {
  asm volatile("buffer_load_dword %0, %1, %2, %3 offen sc1" CNT : "=v"(r) : "v"(voff), "s"(rs), "s"(soff), [id] "i"(ID) : "memory");
}
template <int ID> DI void ld2(u32x2& r, i32x4 rs, unsigned voff, unsigned soff) {
  asm volatile("buffer_load_dwordx2 %0, %1, %2, %3 offen" CNT : "=v"(r) : "v"(voff), "s"(rs), "s"(soff), [id] "i"(ID) : "memory");
}
template <int ID> DI void ldh(unsigned& r, i32x4 rs, unsigned voff) {
  asm volatile("buffer_load_ushort %0, %1, %2, 0 offen" CNT : "=v"(r) : "v"(voff), "s"(rs), [id] "i"(ID) : "memory");
}
template <int ID, int OFF> DI void st4_sc1(u32x4 v, i32x4 rs, unsigned voff, unsigned soff) {
  asm volatile("buffer_store_dwordx4 %0, %1, %2, %3 offen offset:%c4 sc1\n\ts_nop 1" CNT :: "v"(v), "v"(voff), "s"(rs), "s"(soff), "i"(OFF), [id] "i"(ID) : "memory");
}
#define WAITS ".if (vmc-vmk%c[id]) < 63\n\ts_waitcnt vmcnt(vmc-vmk%c[id])\n\t.endif"
template <int ID> DI void need() { asm volatile(WAITS :: [id] "i"(ID) : "memory"); }
template <int ID, class T> DI void need(T& a) { asm volatile(WAITS : "+v"(a) : [id] "i"(ID) : "memory"); }
template <int ID, class T> DI void need(T& a, T& b) { asm volatile(WAITS : "+v"(a), "+v"(b) : [id] "i"(ID) : "memory"); }
DI unsigned opq(unsigned v) { asm volatile("" : "+v"(v)); return v; }
template <class T> DI void pin(T& v) { asm volatile("" : "+v"(v)); }
DI void fence() { __builtin_amdgcn_sched_barrier(0); }
DI void bar_nolds() { fence(); asm volatile("s_barrier" ::: "memory"); fence(); }
DI void bar() { fence(); asm volatile("s_waitcnt lgkmcnt(0)\n\ts_barrier" ::: "memory"); fence(); }
DI u32x4 lds128(unsigned a) { return *(LDSP u32x4*)a; }
DI f32x4 lds128f(unsigned a) { return *(LDSP f32x4*)a; }
DI f32x2 lds64f(unsigned a) { return *(LDSP f32x2*)a; }
DI u32x4 ldstr(unsigned a0, unsigned a1) {
  s16x4 lo = __builtin_amdgcn_ds_read_tr16_b64_v4i16((LDSP s16x4*)a0);
  s16x4 hi = __builtin_amdgcn_ds_read_tr16_b64_v4i16((LDSP s16x4*)a1);
  return __builtin_bit_cast(u32x4, __builtin_shufflevector(lo, hi, 0, 1, 2, 3, 4, 5, 6, 7));
}
DI f32x16 mfma(u32x4 a, u32x4 b, f32x16 c) {
  c = __builtin_amdgcn_mfma_f32_32x32x16_bf16(__builtin_bit_cast(bf16x8, a), __builtin_bit_cast(bf16x8, b), c, 0, 0, 0);
  pin(c);
  return c;
}
DI f32x4 mfma16(u32x4 a, u32x4 b, f32x4 c) {
  c = __builtin_amdgcn_mfma_f32_16x16x32_bf16(__builtin_bit_cast(bf16x8, a), __builtin_bit_cast(bf16x8, b), c, 0, 0, 0);
  pin(c);
  return c;
}
DI unsigned pk(float lo, float hi) {
  f32x2 p = {lo, hi};
  return __builtin_bit_cast(unsigned, __builtin_convertvector(p, bf16x2));
}
DI float bf2f(unsigned v) { return __uint_as_float(v << 16); }
DI float hi2f(unsigned v) { return __uint_as_float(v & 0xffff0000u); }
DI float halfsum(float a, float b) {
  auto r = __builtin_amdgcn_permlane32_swap(__float_as_uint(a), __float_as_uint(b), false, false);
  return __uint_as_float(r[0]) + __uint_as_float(r[1]);
}
template <int CTRL, int RM> DI float dadd(float v) {
  return v + __builtin_bit_cast(float, __builtin_amdgcn_update_dpp(0, __builtin_bit_cast(int, v), CTRL, RM, 0xf, true));
}
DI float hsum(float v) {
  v = dadd<0x111, 0xf>(v); v = dadd<0x112, 0xf>(v); v = dadd<0x114, 0xf>(v); v = dadd<0x118, 0xf>(v);
  return dadd<0x142, 0xa>(v);
}
DI float sum4(f32x4 v) { return (v[0] + v[1]) + (v[2] + v[3]); }
DI unsigned split_hl(float v) { const unsigned p = pk(v, v); return pk(v, v - hi2f(p)); }
DI u32x4 split_op(float v) { const unsigned p = pk(v, v); const float l = v - hi2f(p); return u32x4{p, pk(l, l), 0u, 0u}; }
DI unsigned tau(unsigned r) { return 16u * ((r >> 2) & 1u) + (r & 3u) + 4u * (r >> 3); }
DI unsigned swap23(unsigned r) { return (r & 0x13u) | ((r & 4u) << 1) | ((r & 8u) >> 1); }
DI f32x2 cmul(f32x2 a, f32x2 b) { return f32x2{a[0] * b[0] - a[1] * b[1], a[0] * b[1] + a[1] * b[0]}; }
DI f32x2 cmad(f32x2 a, f32x2 b, f32x2 c) { return f32x2{a[0] * b[0] + (c[0] - a[1] * b[1]), a[0] * b[1] + (c[1] + a[1] * b[0])}; }
DI f32x2 csq(f32x2 a) { return f32x2{a[0] * a[0] - a[1] * a[1], 2.0f * a[0] * a[1]}; }
constexpr unsigned ONES = 0x3F803F80u;
template <int FROM, int TO> DI void rebase(unsigned& a) {
  constexpr int d = (int)WB(TO) - (int)WB(FROM);
  if constexpr (d != 0) asm volatile("v_add_u32 %0, %1, %0" : "+v"(a) : "i"(d));
}
template <int I, int N, class Fn> DI void static_for(Fn&& f) {
  if constexpr (I < N) { f(IC<I>{}); static_for<I + 1, N>(f); }
}

DI void want_flags(unsigned& rdy, unsigned mask, unsigned tag, i32x4 rs, unsigned voff, unsigned soff) {
  if ((rdy & mask) == mask) return;
  for (int it = 0; it < SPIN_CAP; ++it) {
    unsigned w;
    asm volatile("s_sleep 1\n\tbuffer_load_dword %0, %1, %2, %3 offen sc1\n\ts_waitcnt vmcnt(0)" : "=&v"(w) : "v"(voff), "s"(rs), "s"(soff) : "memory");
    rdy = (unsigned)__builtin_amdgcn_ballot_w64(w == tag);
    if ((rdy & mask) == mask) return;
  }
}

template <class Fn> DI void wait_gran(u32x4& g, bool skip, unsigned tag, i32x4 rs, Fn voff_fn, unsigned soff) {
  if (__builtin_amdgcn_ballot_w64(!skip && ((g[1] ^ tag) | (g[3] ^ tag)) != 0u) == 0ull) return;
  const unsigned voff = voff_fn();
  for (int it = 0; it < SPIN_CAP; ++it) {
    __builtin_amdgcn_s_sleep(2);
    asm volatile("buffer_load_dwordx4 %0, %1, %2, %3 offen sc1\n\ts_waitcnt vmcnt(0)" : "=&v"(g) : "v"(voff), "s"(rs), "s"(soff) : "memory");
    if (__all(skip || (g[1] == tag && g[3] == tag))) return;
  }
}
template <class Fn> DI void wait_gran2(u32x4& g, u32x4& k, bool skg, bool skk, unsigned tag, i32x4 rs, Fn voff_fn, unsigned sg, unsigned sk) {
  auto bad = [&] { return (skg ? 0u : (g[1] ^ tag) | (g[3] ^ tag)) | (skk ? 0u : (k[1] ^ tag) | (k[3] ^ tag)); };
  if (__builtin_amdgcn_ballot_w64(bad() != 0u) == 0ull) return;
  const u32x2 v = voff_fn();
  for (int it = 0; it < SPIN_CAP; ++it) {
    __builtin_amdgcn_s_sleep(2);
    asm volatile("buffer_load_dwordx4 %0, %2, %4, %5 offen sc1\n\tbuffer_load_dwordx4 %1, %3, %4, %6 offen sc1\n\ts_waitcnt vmcnt(0)"
                 : "=&v"(g), "=&v"(k) : "v"(v[0]), "v"(v[1]), "s"(rs), "s"(sg), "s"(sk) : "memory");
    if (__builtin_amdgcn_ballot_w64(bad() != 0u) == 0ull) return;
  }
}
DI float exp_acc(float x) {
  const float t = x * 1.44269504088896341f;
  return __builtin_amdgcn_exp2f(t) * (1.0f + 0.693147181f * (fmaf(x, 1.44269504088896341f, -t) + x * 1.92596299e-8f));
}

struct GluK { float k1, k2, iv; };
DI unsigned glu_pair(const f32x16& a0, const f32x16& a1, int e, const GluK& k) {
  const f32x2 a = {a0[e], a0[e + 1]}, u = {a1[e], a1[e + 1]};
  const f32x2 z = a * ((a * a) * k.k2 + k.k1);
  const f32x2 d = f32x2{__builtin_amdgcn_exp2f(z[0]), __builtin_amdgcn_exp2f(z[1])} * k.iv + k.iv;
  const f32x2 o = (a * u) * f32x2{__builtin_amdgcn_rcpf(d[0]), __builtin_amdgcn_rcpf(d[1])};
  return pk(o[0], o[1]);
}

constexpr int PROC = 4;
constexpr int RSP = 2;
constexpr int GS_LD = 40, GS_CK = 57;
constexpr int KS = 8, KC = 11;
constexpr int GG_LD = 79;
constexpr int S2 = 1, SSP = 2;
constexpr int KE = 15;
constexpr int T3_K = 6;
constexpr int PRIO_Q = 16;
constexpr int WS0 = 72, WS1 = 30, WD0 = 3, WD1 = 7, PER0 = 2, PER1 = 5;
constexpr int WLATE = 4, WL_U = 2;
constexpr int DA = 2;
constexpr int U_L = 3;
constexpr int UB0 = 6, UB1 = 5;
constexpr int bar_step(int T, int k) { return T ? 16 * (k - 8) + 12 : 8 * k + 4; }
constexpr int bar0_at(int g) { for (int k = 0; k < 16; ++k) if (bar_step(0, k) == g) return k; return -1; }
constexpr int rel(int k) { return k == 15 ? 15 : k - 1; }
constexpr int CORD[8] = {2, 3, 7, 0, 1, 4, 5, 6};
struct Plan { int wf[128], cf[128], wr[128], nwf, nwr, nc1, nc2; };
constexpr Plan make_plan(int T) {
  Plan s{};
  constexpr int need_c[8] = {0, 0, 12, 13, 13, 14, 15, 15};
  const int per = T ? PER1 : PER0, lat = T ? 16 : 8, nw = (T ? 10 : 12) - WLATE, t0 = T ? WS1 : WS0, dt = T ? WD1 : WD0;
  int w = 0, c1 = 0, c2 = 4, done = -1, rd[12] = {};
  for (int t = 0; t < 128; ++t) { s.wf[t] = -1; s.cf[t] = -1; s.wr[t] = -1; }
  for (int t = t0; t < 128; ++t) {
    for (int k = 8; k < 16; ++k) if (bar_step(T, k) == t) done = rel(k);
    if (t % per != 1 % per && done < 15) continue;
    const int pw = w >> 1;
    if (w < 2 * nw && ((w & 1) || pw < 3 || rd[pw - 3] + 2 <= t)) {
      {
        s.wf[t] = w;
        if (w & 1) {
          rd[pw] = t + lat > t0 + lat + dt * pw ? t + lat : t0 + lat + dt * pw;
          if (rd[pw] < 128) { s.wr[rd[pw]] = pw; ++s.nwr; }
        }
        ++w;
      }
    } else if (w == 2 * nw && c1 < 4 && rd[nw - 1] + 2 <= t) s.cf[t] = c1++;
    else if (c2 < 16 && done >= need_c[c2 >> 1]) s.cf[t] = c2++;
  }
  s.nwf = w; s.nc1 = c1; s.nc2 = c2;
  return s;
}
constexpr Plan PLAN[2] = {make_plan(0), make_plan(1)};

struct Args { const u16 *x, *lre, *lim, *Bre, *Bim, *Cre, *Cim, *ldt, *Dsk, *wg, *wu, *wd; float* out; char* ws; };

template <bool SSM>
DI void body(const Args& p, const unsigned rt, const unsigned s, const unsigned rg) {
  constexpr int Z = SSM ? 1000 : 0;
  constexpr unsigned cg = SSM ? 0u : 1u;
  constexpr int TY = SSM ? 0 : 1;
  asm volatile(".set vmc, 0");
  const unsigned lane = threadIdx.x & 63u, wv = cg * 4u + rg;
  const unsigned r = lane & 31u, h = lane >> 5;

  const i32x4 rsX = mk_rsrc(p.x, SEQ * DM * 2);
  const i32x4 rsWD = mk_rsrc(p.wd, FF * DM * 2), rsCr = mk_rsrc(p.Cre, DM * DM * 2), rsCi = mk_rsrc(p.Cim, DM * DM * 2);
  const i32x4 rsWS = mk_rsrc(p.ws, WS_END);
  const unsigned ldsw = wv * 1024u;
  const unsigned u0 = (43u * s) >> 4, nu = ((43u * s + 43u) >> 4) - u0;
  const unsigned sX = rt * (128u * 1024u), sB = s * 32768u;
  const unsigned rt2 = (blockIdx.x & 7u) * 2u + ((blockIdx.x >> 3) & 1u), ct = blockIdx.x >> 5, rh = (blockIdx.x >> 4) & 1u;
  const unsigned sJ = ct * 65536u, sK = ct * 128u;
  const unsigned cp = lane & 7u;

  const unsigned rk = ((blockIdx.x & 6u) | ((rt ^ s) & 1u)) ^ ((blockIdx.x & 1u) << 2);
  const i32x4 rsBr = mk_rsrc(p.Bre, DM * DM * 2), rsBi = mk_rsrc(p.Bim, DM * DM * 2);
  const i32x4 rsG = mk_rsrc(p.wg, DM * FF * 2), rsU = mk_rsrc(p.wu, DM * FF * 2);
  const unsigned row = 8u * rg + (lane >> 3), sw = (cp ^ ((row >> 1) & 7u)) * 16u;
  const unsigned vX = tau(row) * 1024u + sw, vB = swap23(row) * 1024u + sw;
  const unsigned vG = (16u * rg + (lane >> 2)) * (FF * 2u) + (lane & 3u) * 16u;
  auto fill1 = [&](auto MM) {
    constexpr int C = MM / 6;
    constexpr unsigned I = MM % 6;
    constexpr int ID = Z + ID_F + C;
    if constexpr (C < 8) {
      if constexpr (I < 4) glds_x<ID, C * 128u + I * 32768u, SB(C) + I * 4096u>(rsX, vX, sX + rk * 128u, rg * 1024u);
      else glds_x<ID, C * 128u, SB(C) + I * 4096u>(I == 4 ? rsBr : rsBi, vB, sB + rk * 128u, rg * 1024u);
    } else {
      glds_m<ID, C - 8, 64u * FF * 2u, SB(C) + (I % 3) * 8192u + (I / 3) * 4096u>(I < 3 ? rsG : rsU, vG, (u0 + I % 3) * 64u, rk, rg * 1024u);
    }
  };
  constexpr int NW = SSM ? 12 : 10;
  constexpr int NL = NW - WLATE;
  auto wpos = [](int w) { return w < NL ? 6 + w % 3 : 8 - (w - NL); };
  unsigned vWD, vC, vC1;
  const unsigned sD = sK + wv * 32768u, sC = sJ + wv * 128u;
  auto fillW = [&](auto MM) {
    constexpr unsigned w = MM >> 1, T = MM & 1;
    if constexpr (w < NW) glds<Z + ID_W + w, (256u * (w >> 1) + 16u * (w & 1u) + 8u * T) * 1024u, RB(wpos(w)) + T * 8192u>(rsWD, vWD, sD, ldsw);
  };
  u32x4 bw[12][2];
  unsigned pa0, pa1;
  auto rdW = [&](auto WW) {
    constexpr int w = WW;
    if constexpr (w < NW) {
      need<Z + ID_W + w>();
      constexpr unsigned o = (wpos(w) - 5) * 16384u;
      bw[w][0] = ldstr(pa0 + o, pa0 + o + 8192u);
      bw[w][1] = ldstr(pa1 + o, pa1 + o + 8192u);
    }
  };
  auto held = [&](auto WW) { if constexpr (WW < NW) { pin(bw[WW][0]); pin(bw[WW][1]); } };
  auto fillC = [&](auto MM) {
    constexpr unsigned c = CORD[MM >> 1], T = MM & 1;
    if constexpr (MM == 0) { held(IC<NL - 3>{}); held(IC<NL - 2>{}); held(IC<NL - 1>{}); }
    glds<Z + ID_C + (MM >> 1), (16u * (c & 3u) + 8u * T) * 1024u, RB(c ^ 4u) + T * 8192u>(c < 4 ? rsCr : rsCi, T ? vC1 : vC, sC, ldsw);
  };
  auto w2_addr = [&](unsigned ln) {
    const u32x2 t = *(LDSP u32x2*)(L_RA + 512u + ln * 8u);
    vWD = t[0] & 0xffffu; vC = t[1]; vC1 = vC ^ 64u; pa0 = RB(5) + ldsw + (t[0] >> 16); pa1 = pa0 ^ 64u;
    pin(vWD); pin(vC); pin(vC1); pin(pa0); pin(pa1);
  };
  auto w2_step = [&](auto TT) {
    constexpr int T = TT;
    if constexpr (PLAN[TY].wr[T] >= 0) rdW(IC<(PLAN[TY].wr[T] >= 0 ? PLAN[TY].wr[T] : 0)>{});
    if constexpr (PLAN[TY].wf[T] >= 0) {
      constexpr int m = PLAN[TY].wf[T] >= 0 ? PLAN[TY].wf[T] : 0;
      if constexpr (m >= 6 && (m & 1) == 0) held(IC<(m >= 6 ? m / 2 - 3 : 0)>{});
      fillW(IC<m>{});
    }
    if constexpr (PLAN[TY].cf[T] >= 0) fillC(IC<(PLAN[TY].cf[T] >= 0 ? PLAN[TY].cf[T] : 0)>{});
  };

  unsigned plr, pli, pdt, epo;
  u32x4 xr0, xr1, br;
  asm volatile("s_nop 4" ::: "memory");
  if constexpr (SSM) {
    ld1_sc1<Z + ID_ROW>(epo, rsWS, 0u, WS_EPO);
    ld4<Z + ID_ROW>(xr0, rsX, h * 1024u + r * 16u, sX + (s * 8u + rg * 2u) * 1024u);
    ld4<Z + ID_ROW>(xr1, rsX, h * 1024u + r * 16u, sX + (s * 8u + rg * 2u) * 1024u + 512u);
    ld4<Z + ID_ROW>(br, (rg & 1u) ? rsBi : rsBr, lane * 16u, sB + (rt * 2u + (rg >> 1)) * 1024u);
    const unsigned po = (32u * s + swap23(r)) * 2u;
    ldh<Z + ID_PAR>(plr, mk_rsrc(p.lre, 1024), po); ldh<Z + ID_PAR>(pli, mk_rsrc(p.lim, 1024), po); ldh<Z + ID_PAR>(pdt, mk_rsrc(p.ldt, 1024), po);
  } else {
    static_for<0, 6>(fill1);
  }
  if constexpr (!SSM) static_for<6, 6 * PROC>(fill1);

  unsigned xa[4], wa[4], gb;
  {
    const unsigned t = WB(0) + 128u * r + 16u * ((2u * rg + h) ^ ((r >> 1) & 7u));
    gb = WB(8) + 64u * (16u * rg + 8u * h + ((lane & 15u) >> 2)) + 32u * ((lane >> 4) & 1u) + 8u * (lane & 3u);
    pin(gb);
#pragma unroll
    for (unsigned i = 0; i < 4; ++i) {
      xa[i] = (t ^ (i << 5)) + rg * 4096u; pin(xa[i]);
      if constexpr (SSM) { wa[i] = (t ^ (i << 5)) + 16384u; pin(wa[i]); }
    }
  }
  u32x4 sel0, sel1;
  auto mksel = [&](unsigned ln, unsigned row, u32x4& sel) {
    const unsigned t = (ln & 15u) == row + ((ln >> 2) & 4u) ? ONES : 0u;
    sel = u32x4{t, t, t, t}; pin(sel);
  };
  u32x4 xf[32];
  GluK gk;
  unsigned opM = 0u, poll, tag, vS0 = tau(r) * 16u;
  f32x16 b0 = {}, b1 = {}, b2 = {}, b3 = {};
  f32x4 cs4 = {}, cs5 = {}, cq[4];
  u32x4 o0, o1, g0, g1;
  const unsigned hrow = (rt * 4u + rg) * NQ;
  const unsigned mbw = WS_MBOX + rt * 2048u + s * 4u, mbr = WS_MBOX + rt2 * 2048u + (ct * 2u + rh) * 128u;
  const unsigned lane0 = lane;
  pin(vS0);
  auto row_consts = [&](unsigned hh) {
    opM = hh ? 0u : split_hl(-__uint_as_float(g0[0]));
    gk.iv = __uint_as_float(g0[2]);
    const float ra = __builtin_amdgcn_rsqf(gk.iv);
    gk.k1 = -2.302208198f * ra;
    gk.k2 = -0.1029432397f * (ra * ra * ra);
    pin(opM); pin(gk.k1); pin(gk.k2); pin(gk.iv);
    return ra;
  };

  if constexpr (SSM) {
    f32x16 a0 = {}, a1 = {}, zr, zi;
    f32x4 rs[4], wj;
    f32x2 e1, pre, lam, cf, l16, wseg, ya, sg[7];
    u32x4 w0[2], w1[2], opA, opB, id0, id1, pr0, pi0, pr1, pi1;
    unsigned rsa, vS1 = swap23(r) * 16u;
    const unsigned gso = WS_AGG + s * 8192u;
    constexpr int GS = bar_step(0, KS), GC = bar_step(0, KC), P1 = GS - 4, GE = GC + 4 + 8 * S2;
    pin(vS1);
    {
      const unsigned q4 = (lane & 15u) >> 2, rw = lane >> 3, hs = (((cp >> 2) ^ ((rw >> 1) & 1u)) * 4u + (cp & 3u)) * 16u;
      const unsigned pl = 128u * (4u * h + q4) + 64u * (q4 >> 1) + 32u * ((lane >> 4) & 1u) + 8u * (lane & 3u);
      *(LDSP u32x2*)(L_RA + 512u + lane * 8u) = u32x2{(rw * 1024u + hs) | (pl << 16), rw * 1024u + (cp ^ (lane >> 4)) * 16u};
      const unsigned d = r - 4u * h, idv = 0x3F80u << (16u * (d & 1u)), idq = (d & 36u) ? 99u : ((d & 2u) >> 1) + 2u * (d >> 3);
      u32x4 v0, v1;
#pragma unroll
      for (unsigned q = 0; q < 4; ++q) { v0[q] = idq == q ? idv : 0u; v1[q] = idq == q + 4u ? idv : 0u; }
      *(LDSP u32x4*)(L_ID + lane * 16u) = v0; *(LDSP u32x4*)(L_ID + 1024u + lane * 16u) = v1;
      *(LDSP f32x2*)(L_SEG + (rg * 64u + lane) * 8u) = f32x2{0.f, 0.f};
    }
    fence();
    {
      asm volatile(WAITS : "+v"(xr0), "+v"(xr1), "+v"(br), "+v"(epo) : [id] "i"(Z + ID_ROW) : "memory");
      tag = __builtin_amdgcn_readfirstlane(epo) + 1u;
      *(LDSP unsigned*)(L_CSP + 1024u) = tag;
      float v[16], sm = 0.f, sb = 0.f;
#pragma unroll
      for (int e = 0; e < 4; ++e) {
        v[4 * e] = bf2f(xr0[e]); v[4 * e + 1] = hi2f(xr0[e]); v[4 * e + 2] = bf2f(xr1[e]); v[4 * e + 3] = hi2f(xr1[e]);
        sm += (v[4 * e] + v[4 * e + 1]) + (v[4 * e + 2] + v[4 * e + 3]);
        sb += bf2f(br[e]) + hi2f(br[e]);
      }
      sm = hsum(sm);
      const float m0 = __builtin_bit_cast(float, __builtin_amdgcn_readlane(__builtin_bit_cast(int, sm), 31));
      const float m1 = __builtin_bit_cast(float, __builtin_amdgcn_readlane(__builtin_bit_cast(int, sm), 63));
      const float m = (h ? m1 : m0) * (1.0f / DM);
      float q0 = 0.f, q1 = 0.f;
#pragma unroll
      for (int e = 0; e < 8; ++e) { const float d0 = v[2 * e] - m, d1 = v[2 * e + 1] - m; q0 = fmaf(d0, d0, q0); q1 = fmaf(d1, d1, q1); }
      const float iv = hsum(q0 + q1) * (1.0f / DM) + 1e-5f;
      sb = dadd<0x143, 0xc>(hsum(sb));
      st4_sc1<Z + ID_M, 0>(u32x4{__float_as_uint(m), tag, __float_as_uint(iv), tag}, rsWS, r == 31u ? h * 16u : OOB, WS_STAT + (rt * 128u + s * 8u + rg * 2u) * 16u);
      st2_sc1<Z + ID_M>(u32x2{__float_as_uint(sb), tag}, rsWS, lane == 63u ? 0u : OOB, WS_CSB + (s * 32u + rt * 2u + (rg >> 1)) * 16u + (rg & 1u) * 8u);
    }
    fence();
    {
      asm volatile(WAITS : "+v"(plr), "+v"(pli), "+v"(pdt) : [id] "i"(Z + ID_PAR) : "memory");
      const float dt = bf2f(pk(exp_acc(bf2f(pdt)), 0.f)), Lr = bf2f(plr), Li = bf2f(pli);
      const float ex = exp_acc(dt * Lr), la = dt * Lr * 1.44269504088896341f;
      const float th = dt * Li, rv = th * 0.15915494309189535f;
      const float fr = (rv - __builtin_rintf(rv)) + (fmaf(th, 0.15915494309189535f, -rv) + th * 6.4206382e-9f);
      auto powl = [&](float n) {
        const float m = __builtin_amdgcn_exp2f(n * la), ph = __builtin_amdgcn_fractf(n * fr);
        return f32x2{m * __builtin_amdgcn_cosf(ph), m * __builtin_amdgcn_sinf(ph)};
      };
      lam = f32x2{ex * __builtin_amdgcn_cosf(fr), ex * __builtin_amdgcn_sinf(fr)};
      const float den = Lr * Lr + Li * Li, q = __builtin_amdgcn_rcpf(den), inv = 2.0f * fmaf(fmaf(-den, q, 1.0f), q, q), nr = lam[0] - 1.0f;
      const int j1 = 2 * (int)rg + (int)h;
      const f32x2 w1_ = powl(128.0f * fmaxf((float)((int)rt - 1 - j1), 0.0f)), w2_ = powl(128.0f * fmaxf((float)((int)rt - 9 - j1), 0.0f));
      const f32x2 t16 = powl(16.0f);
      *(LDSP f32x4*)(L_WJ + (rg * 64u + lane) * 16u) = f32x4{w1_[0], w1_[1], w2_[0], w2_[1]};
      *(LDSP f32x2*)(L_WJ + 4096u + (rg * 64u + lane) * 8u) = powl(16.0f * (float)j1);
      *(LDSP f32x4*)(L_CSP + r * 16u) = f32x4{t16[0], t16[1], (nr * Lr + lam[1] * Li) * inv, (lam[1] * Lr - nr * Li) * inv};
      pin(lam);
    }
    auto reads = [&](auto NN, auto MM) {
      constexpr int N = NN, M = MM, C = N >> 2, i = N & 3;
      if constexpr (M == 0) {
        if constexpr (C < 8) { rebase<(C ? C - 1 : 0), C>(xa[i]); xf[N] = lds128(xa[i] + RO(C)); }
        if constexpr (C == 8) wa[i] = gb ^ (unsigned)(i << 10); else rebase<(C ? C - 1 : 0), C>(wa[i]);
      }
      u32x4& w = M ? w1[N & 1] : w0[N & 1];
      if constexpr (C >= 8) w = ldstr(wa[i] + RO(C) + M * 4096u, wa[i] + RO(C) + M * 4096u + 256u);
      else w = lds128(wa[i] + RO(C) + M * 4096u);
    };
    bar();
    reads(IC<0>{}, IC<0>{}); reads(IC<0>{}, IC<1>{}); reads(IC<1>{}, IC<0>{}); reads(IC<1>{}, IC<1>{});
    static_for<0, 128>([&](auto GG) {
      constexpr int G = GG, N = G >> 1, M = G & 1, C = N >> 2, i = N & 3;
      fence();
      {
        const u32x4 x = xf[N & 31], w = M ? w1[N & 1] : w0[N & 1];
        if constexpr (C < 8) { f32x16& acc = M ? a1 : a0; acc = mfma(x, w, acc); }
        else { f32x16& acc = M ? b1 : b0; acc = mfma(w, x, acc); }
        if constexpr (C >= 8 && i == (C <= KS ? 3 : 0)) cs4 = mfma16(M ? sel1 : sel0, w, cs4);
        if constexpr (G == GS_CK + 5) { const u32x4 m = {opM, opM, 0u, 0u}; a0 = mfma(m, opA, a0); a1 = mfma(m, opB, a1); }
        if constexpr (G == GC + 3 + 4 * S2) { zr = mfma(pr0, id0, f32x16{}); zi = mfma(pi0, id0, f32x16{}); }
        if constexpr (G == GC + 3 + 8 * S2) { zr = mfma(pr1, id1, zr); zi = mfma(pi1, id1, zi); }
      }
      fence();
      auto part = [&](const unsigned lane) {
        const unsigned r = lane & 31u, h = lane >> 5, j1 = 2u * rg + h, j2 = j1 + 8u;
        constexpr int K = bar0_at(G);
        if constexpr (K >= 0) {
          if constexpr (K == KE) need<Z + ID_STE>();
          if constexpr (K == KS || K == KC || K == 15) bar(); else bar_nolds();
          if constexpr (K == 15) { cq[0] = lds128f(L_CSP + r * 16u); cq[1] = lds128f(L_CSP + 512u + r * 16u); }
          if constexpr (K == KS) {
#pragma unroll
            for (int t = 0; t < 7; ++t) sg[t] = lds64f(L_SEG + t * 256u + rg * 512u + lane * 8u);
          }
          if constexpr (K == KC) { g0 = lds128(L_CAR + r * 32u); g1 = lds128(L_CAR + r * 32u + 16u); }
        }
        if constexpr (N + 2 < 64) reads(IC<(N + 2 < 64 ? N + 2 : 0)>{}, IC<M>{});
        if constexpr (G == WS0 - 1) w2_addr(lane);
        w2_step(GG);
        if constexpr (G == GS_LD) {
          ld4_sc1<Z + ID_S, 0>(g0, rsWS, vS0, WS_STAT + (rt * 128u + rg * 32u) * 16u);
          ld4_sc1<Z + ID_S, 0>(g1, rsWS, vS1, WS_CSB + s * 512u);
        }
        if constexpr (G == GS_CK) {
          need<Z + ID_S>(g0, g1);
          wait_gran2(g0, g1, false, false, tag, rsWS, [&] { return u32x2{vS0, vS1}; }, WS_STAT + (rt * 128u + rg * 32u) * 16u, WS_CSB + s * 512u);
        }
        if constexpr (G == GS_CK + 1) {
          *(LDSP float*)(L_RA + rg * 128u + (vS0 >> 2)) = row_consts(h);
          *(LDSP u32x2*)(L_CAR + (rg * 32u + r) * 8u) = u32x2{g0[0], g0[2]};
          asm volatile("" ::: "memory");
        }
        if constexpr (G == GS_CK + 3) { opA = split_op(__uint_as_float(g1[0])); pin(opA); }
        if constexpr (G == GS_CK + 4) { opB = split_op(__uint_as_float(g1[2])); pin(opB); }
        static_assert(GS_CK + 7 <= P1 && P1 >= 64 && KS < 10, "first scan pass");
        if constexpr (G == 67) mksel(lane, 0u, sel0);
        if constexpr (G == 69) mksel(lane, 8u, sel1);
        if constexpr (G == P1 - 2) { rsa = L_RA + (rg * 32u + 16u * h) * 4u; pin(rsa); }
        if constexpr (G >= P1 - 2 && G < P1 + 2) rs[G - P1 + 2] = lds128f(rsa + 16u * (G - P1 + 2));
        if constexpr (G >= P1 && G < P1 + 4) {
#pragma unroll
          for (int t = 4 * (G - P1); t < 4 * (G - P1) + 4; ++t) {
            a0[t] *= rs[t >> 2][t & 3]; a1[t] *= rs[t >> 2][t & 3];
            if (t == 0) e1 = f32x2{a0[0], a1[0]}; else e1 = cmad(lam, e1, f32x2{a0[t], a1[t]});
          }
          pin(e1);
        }
        if constexpr (G == GS - 1) { *(LDSP f32x2*)(L_SEG + 7u * 256u + rg * 512u + lane * 8u) = e1; l16 = lds64f(L_CSP + r * 16u); }
        if constexpr (G == GS + 1) { pre = cmad(l16, cmad(l16, cmad(l16, sg[0], sg[1]), sg[2]), sg[3]); pin(pre); }
        if constexpr (G == GS + 2) {
          pre = cmad(l16, cmad(l16, cmad(l16, pre, sg[4]), sg[5]), sg[6]); pin(pre);
          e1 = cmad(l16, pre, e1);
          st4_if(rg ^ 3u, u32x4{__float_as_uint(e1[0]), tag, __float_as_uint(e1[1]), tag}, rsWS, h ? (rt * 32u + r) * 16u : OOB, gso);
        }
        if constexpr (G == GG_LD) {
          ld4_sc1<Z + ID_G, 0>(g0, rsWS, j1 < rt ? (rg * 64u + lane) * 16u : OOB, gso);
          ld4_sc1<Z + ID_G, 0>(g1, rsWS, j2 < rt ? (rg * 64u + lane) * 16u : OOB, gso + 4096u);
        }
        static_assert(GE + 3 * SSP < bar_step(0, KE) && GG_LD < GC - 4 && GG_LD > GS + 2, "carry schedule");
        if constexpr (G == GC - 6) { wj = lds128f(L_WJ + (rg * 64u + lane) * 16u); wseg = lds64f(L_WJ + 4096u + (rg * 64u + lane) * 8u); }
        if constexpr (G == GC - 3) {
          need<Z + ID_G>(g0, g1);
          wait_gran2(g0, g1, j1 >= rt, j2 >= rt, tag, rsWS,
                     [&] { return u32x2{j1 < rt ? (rg * 64u + lane) * 16u : OOB, j2 < rt ? (rg * 64u + lane) * 16u : OOB}; }, gso, gso + 4096u);
        }
        if constexpr (G == GC - 2) {
          l16 = cmad(f32x2{wj[0], wj[1]}, f32x2{__uint_as_float(g0[0]), __uint_as_float(g0[2])},
                     cmul(f32x2{wj[2], wj[3]}, f32x2{__uint_as_float(g1[0]), __uint_as_float(g1[2])}));
          pin(l16);
        }
        if constexpr (G == GC - 1) { *(LDSP float*)(L_CAR + r * 32u + rg * 8u + h * 4u) = halfsum(l16[0], l16[1]); cf = lds64f(L_CSP + 8u + r * 16u); }
        if constexpr (G == GC + 3) { id0 = lds128(L_ID + lane * 16u); id1 = lds128(L_ID + 1024u + lane * 16u); }
        if constexpr (G == GC + 2) {
          const f32x2 cin = {(__uint_as_float(g0[0]) + __uint_as_float(g0[2])) + (__uint_as_float(g1[0]) + __uint_as_float(g1[2])),
                             (__uint_as_float(g0[1]) + __uint_as_float(g0[3])) + (__uint_as_float(g1[1]) + __uint_as_float(g1[3]))};
          e1 = cmad(wseg, cin, pre); pin(e1);
        }
        if constexpr (G >= GC + 3 && G < GC + 3 + 8 * S2) {
          constexpr int q = (G - GC - 3) / S2, ph = (G - GC - 3) % S2;
          if constexpr (ph == 0) { e1 = cmad(lam, e1, f32x2{a0[2 * q], a1[2 * q]}); ya = cmul(cf, e1); pin(e1); pin(ya); }
          if constexpr (ph == S2 - 1) {
            e1 = cmad(lam, e1, f32x2{a0[2 * q + 1], a1[2 * q + 1]});
            const f32x2 yb = cmul(cf, e1);
            unsigned vr = pk(ya[0], yb[0]), vi = pk(-ya[1], -yb[1]);
            pin(e1); pin(vr); pin(vi);
            (q < 4 ? pr0 : pr1)[q & 3] = vr; (q < 4 ? pi0 : pi1)[q & 3] = vi;
          }
        }
        if constexpr (G >= GE && G < GE + 4 * SSP && (G - GE) % SSP == 0) {
          constexpr int q = (G - GE) / SSP;
          const f32x16& z = q < 2 ? zr : zi;
#pragma unroll
          for (int e = 0; e < 4; ++e) o0[e] = pk(z[8 * (q & 1) + 2 * e], z[8 * (q & 1) + 2 * e + 1]);
          st4_sc1<Z + ID_STE, 1024 * (q & 1)>(o0, rsWS, lane * 16u, WS_H + (hrow + 4u * (s >> 1) + (s & 1u) + (q & 2)) * 2048u);
        }
        if constexpr (G == 122) *(LDSP float*)(L_CSP + lane * 16u + rg * 4u) = cs4[0];
      };
      constexpr bool USE_LANE = G == 124 || G == 122 || G == GS || G == GS - 1 || G == GS + 2 || G == GS_CK + 1 || G == 67 || G == 69 || G == WS0 - 1 || G == P1 - 2 ||
                                G == GG_LD || G == GC || G == GC - 6 || G == GC - 3 || G == GC - 1 || G == GC + 3 || (G >= GE && G < GE + 4 * SSP && (G - GE) % SSP == 0);
      if constexpr (USE_LANE) part(opq(lane0)); else part(lane0);
    });
  } else {
    u32x4 w[4];
    auto rdw = [&](auto NN, auto MM) {
      constexpr int N = NN, M = MM, C = 8 + (N >> 2), i = N & 3;
      if constexpr (M == 0) { if constexpr (C == 8) wa[i] = gb ^ (unsigned)(i << 10); else rebase<C - 1, C>(wa[i]); }
      w[M] = ldstr(wa[i] + RO(C) + 8192u + M * 4096u, wa[i] + RO(C) + 8192u + M * 4096u + 256u);
    };
    mksel(lane, 0u, sel0); mksel(lane, 8u, sel1);
    need<Z + ID_F>();
    bar();
    tag = __builtin_amdgcn_readfirstlane(*(LDSP unsigned*)(L_CSP + 1024u));
    static_for<0, 8>([&](auto CC) {
      constexpr int C = CC;
#pragma unroll
      for (int i = 0; i < 4; ++i) { rebase<(C ? C - 1 : 0), C>(xa[i]); xf[4 * C + i] = lds128(xa[i] + RO(C)); }
      need<Z + ID_F + C + 1>();
      bar();
      {
        constexpr int lo = C + PROC > 10 ? 10 : C + PROC, hi = C == 7 ? 11 : (lo + 1 > 10 ? 10 : lo + 1);
        static_assert(PROC <= 5, "ring depth");
        static_for<6 * lo, 6 * hi>(fill1);
      }
      if constexpr (C == T3_K) {
        const unsigned ln = opq(lane0), v = rt2 * 2u + rh;
        touch_if(rg, rsWD, ln < 43u ? ln * (32u * 1024u) : OOB, sK + v * 1024u);
        touch_if(rg ^ 1u, (v & 1u) ? rsCi : rsCr, ln < 32u ? ln * 128u : OOB, sJ + (v >> 1) * 4096u);
        touch_if(rg ^ 2u, mk_rsrc(p.Dsk, 1024), ln == 0u ? 0u : OOB, sK);
      }
    });
    static_for<0, 4>([&](auto MM) { rdw(IC<0>{}, MM); });
    static_for<0, 128>([&](auto QQ) {
      constexpr int Q = QQ, N = Q >> 2, M = Q & 3, C = 8 + (N >> 2), i = N & 3;
      fence();
      if constexpr (Q == PRIO_Q) __builtin_amdgcn_s_setprio(1);
      {
        f32x16& acc = M == 0 ? b0 : (M == 1 ? b1 : (M == 2 ? b2 : b3));
        acc = mfma(w[M], xf[N], acc);
        if constexpr (i == 0) { f32x4& cs = M < 2 ? cs4 : cs5; cs = mfma16((M & 1) ? sel1 : sel0, w[M], cs); }
      }
      fence();
      if constexpr ((Q & 15) == 12) {
        if constexpr (C < 15) need<Z + ID_F + (C < 15 ? C + 1 : 0)>();
        if constexpr (C == 15) bar(); else bar_nolds();
        if constexpr (C == KE) st1_if(rg, tag, rsWS, lane0 < 16u ? lane0 * 128u : OOB, mbw);
        if constexpr (C == 15) {
          const unsigned a = (opq(lane0) & 31u) * 16u;
          cq[0] = lds128f(L_CSP + 1024u + a); cq[1] = lds128f(L_CSP + 1536u + a); cq[2] = lds128f(L_SEG + a); cq[3] = lds128f(L_SEG + 512u + a);
        }
        if constexpr (C == KS) { const u32x2 t = *(LDSP u32x2*)(L_CAR + (rg * 32u + (opq(lane0) & 31u)) * 8u); g0[0] = t[0]; g0[2] = t[1]; }
      }
      if constexpr (N + 1 < 32) rdw(IC<(N + 1 < 32 ? N + 1 : 0)>{}, IC<M>{});
      {
        constexpr int K = 8 + (Q - 13) / 16, J = Q >= 13 ? (Q - 13) % 16 : 99;
        if constexpr (J % RSP == 0 && J / RSP < 6 && K + 3 < 16) fill1(IC<(J / RSP < 6 && K + 3 < 16 ? 6 * (K + 3) + J / RSP : 0)>{});
      }
      if constexpr (Q == 16 * (KS - 8) + 20) row_consts(opq(lane0) >> 5);
      if constexpr (Q == WS1 - 2) w2_addr(opq(lane0));
      w2_step(QQ);
      if constexpr (Q == 120) {
        const unsigned a = opq(lane0) * 16u + rg * 4u;
        *(LDSP float*)(L_CSP + 1024u + a) = cs4[0]; *(LDSP float*)(L_SEG + a) = cs5[0];
      }
    });
  }
  fence();
  {
    const u32x4 m = {opM, opM, 0u, 0u};
    b0 = mfma(split_op(sum4(cq[0])), m, b0); b1 = mfma(split_op(sum4(cq[1])), m, b1);
    if constexpr (!SSM) { b2 = mfma(split_op(sum4(cq[2])), m, b2); b3 = mfma(split_op(sum4(cq[3])), m, b3); }
  }
  fence();
  {
  const unsigned lane = opq(lane0), r = lane & 31u, h = lane >> 5, avo = lane * 16u, fvo = r * 4u;
  const unsigned tid = wv * 64u + lane;
  const unsigned aso = WS_H + (rt2 * 4u + rh * 2u) * NQ * 2048u;
  constexpr int NU = SSM ? 10 : 9;
  u32x4 A[10][4], bc[8][2];
  f32x16 c00 = {}, c01 = {}, c10 = {}, c11 = {};
  unsigned rdy;
  auto lda = [&](auto UU) {
    constexpr unsigned U = UU;
    if constexpr (U < NU) {
      if constexpr (U >= 4) {
        const unsigned u = wv + 8u * (U - 4u);
        want_flags(rdy, u < 43u ? 0x10000u << (((16u * u + 15u) * 1525u) >> 16) : 0u, tag, rsWS, fvo, mbr);
      }
      unsigned so = aso + (U < 4 ? 4u * wv + U : 32u + wv + 8u * (U - 4u)) * 2048u;
      if constexpr (U == 9) so = wv < 3u ? so : SOOB;
      ld4_sc1<Z + ID_A + U, 0>(A[U][0], rsWS, avo, so); ld4_sc1<Z + ID_A + U, 1024>(A[U][1], rsWS, avo, so);
      ld4_sc1<Z + ID_A + U, 0>(A[U][2], rsWS, avo, so + NQ * 2048u); ld4_sc1<Z + ID_A + U, 1024>(A[U][3], rsWS, avo, so + NQ * 2048u);
    }
  };
  const unsigned ca = ldsw + 8192u * (r >> 3) + 128u * (r & 7u) + 16u * (h ^ ((r >> 1) & 7u));
  auto rdC = [&](auto UU) {
    constexpr unsigned U = UU;
#pragma unroll
    for (unsigned k = 2 * U; k < 2 * U + 2; ++k) {
      unsigned a = (ca ^ ((k & 3u) << 5)) + RB((k & 4u) ^ 4u);
      pin(a);
      bc[k][0] = lds128(a); bc[k][1] = lds128(a + 32768u);
    }
  };
  auto mm = [&](auto UU) {
    constexpr int U = UU;
    if constexpr (U < NU) {
      fence();
      asm volatile(WAITS : "+v"(A[U][0]), "+v"(A[U][1]), "+v"(A[U][2]), "+v"(A[U][3]) : [id] "i"(Z + ID_A + U) : "memory");
      fence();
      if (U < 9 || wv < 3u) {
#pragma unroll
        for (int k = 0; k < 2; ++k) {
          const u32x4* b = U < 4 ? bc[2 * U + k] : bw[2 * U - 8 + k];
          c00 = mfma(b[0], A[U][k], c00); c01 = mfma(b[0], A[U][2 + k], c01);
          c10 = mfma(b[1], A[U][k], c10); c11 = mfma(b[1], A[U][2 + k], c11);
        }
      }
      fence();
    }
  };
  f32x4 ob[2];
  const unsigned orow = rh * 64u + tau(tid >> 4), ocol = ct * 64u + (tid & 15u) * 4u;
  const unsigned mbv = lane < 16u ? lane * 128u : OOB;

  static_assert(PLAN[TY].nwf == 2 * NL && PLAN[TY].nwr == NL && WLATE <= 4 && UB1 >= 4 && UB0 >= 4, "w_down stream ends inside round B");
  u32x2 dv;
  constexpr int R1 = 4 - PLAN[TY].nc1, R2 = R1 + 16 - PLAN[TY].nc2, NR = R2 + 3, NE = SSM ? 7 : 12;
  auto rest = [&](auto II) {
    constexpr int I = II;
    if constexpr (I < R1) fillC(IC<(I < R1 ? PLAN[TY].nc1 + I : 0)>{});
    else if constexpr (I < R2) fillC(IC<(I < R2 ? PLAN[TY].nc2 + I - R1 : 0)>{});
    else if constexpr (I == R2) glds<Z + ID_X, 0u, L_ID>(rsX, (lane >> 3) * 1024u + (lane & 7u) * 16u, (rt2 * 128u + rh * 64u + wv * 8u) * 1024u + sK, ldsw);
    else if constexpr (I == R2 + 1) { if constexpr (SSM) ld4_sc1<Z + ID_X, 0>(g0, rsWS, avo, WS_STAT + (rt2 * 128u + rh * 64u) * 16u); }
    else ld2<Z + ID_X>(dv, mk_rsrc(p.Dsk, 1024), ocol * 2u, 0u);
  };
  const unsigned hso = WS_H + (hrow + 32u + u0 + cg) * 2048u;
  static_for<0, SSM ? 8 : 16>([&](auto EE) {
    constexpr int E = EE;
    unsigned v = E < 8 ? glu_pair(b0, b1, 2 * (E & 7), gk) : glu_pair(b2, b3, 2 * (E & 7), gk);
    pin(v);
    ((E & 4) ? o1 : o0)[E & 3] = v;
    if constexpr (E < NE) static_for<NR * E / NE, NR * (E + 1) / NE>(rest);
    if constexpr (E == (SSM ? 7 : 15)) ld1_sc1<Z + ID_P2>(poll, rsWS, fvo, mbr);
    if constexpr (!SSM && E == 3) st4_sc1<Z + ID_STL, 0>(o0, rsWS, avo, hso);
    if constexpr (!SSM && E == 7) st4_sc1<Z + ID_STL, 1024>(o1, rsWS, avo, hso);
    if constexpr (!SSM && E == 11) st4_sc1<Z + ID_STL, 0>(o0, rsWS, avo, nu == 3u ? hso + 2048u : SOOB);
    if constexpr (!SSM && E == 15) st4_sc1<Z + ID_STL, 1024>(o1, rsWS, avo, nu == 3u ? hso + 2048u : SOOB);
    if constexpr (SSM && E == 3) st4_sc1<Z + ID_STL, 0>(o0, rsWS, avo, hso);
    if constexpr (SSM && E == 7) st4_sc1<Z + ID_STL, 1024>(o1, rsWS, avo, hso);
    if constexpr ((E & 1)) fence();
  });
  __builtin_amdgcn_s_setprio(0);
  if constexpr (SSM) {
    asm volatile(WAITS : "+v"(g0), "+v"(dv) : [id] "i"(Z + ID_X) : "memory");
    wait_gran(g0, false, tag, rsWS, [&] { return avo; }, WS_STAT + (rt2 * 128u + rh * 64u) * 16u);
    const float ra = __builtin_amdgcn_rsqf(__uint_as_float(g0[2]));
    *(LDSP f32x2*)(L_CAR + lane * 8u) = f32x2{ra, -__uint_as_float(g0[0]) * ra};
  } else need<Z + ID_X>(dv);
  need<Z + ID_P2>(poll);
  rdy = (unsigned)__builtin_amdgcn_ballot_w64(poll == tag);
  want_flags(rdy, 3u << (2u * wv), tag, rsWS, fvo, mbr);
  static_for<0, DA>(lda);
  rdC(IC<0>{});
  need<Z + ID_STL>();
  bar();
  if constexpr (!SSM) st1_if(rg, tag, rsWS, mbv, mbw + 64u);
  auto resid = [&]() {
#pragma unroll
    for (unsigned m = 0; m < 2; ++m) {
      const unsigned tr = tau(tid >> 4) + 32u * m;
      const u32x2 xv = *(LDSP u32x2*)(L_ID + tr * 128u + (tid & 15u) * 8u);
      const f32x2 ab = lds64f(L_CAR + tr * 8u);
#pragma unroll
      for (int e = 0; e < 4; ++e) {
        const float xe = (e & 1) ? hi2f(xv[e >> 1]) : bf2f(xv[e >> 1]);
        const float de = (e & 1) ? hi2f(dv[e >> 1]) : bf2f(dv[e >> 1]);
        ob[m][e] = xe + (xe * ab[0] + ab[1]) * de;
      }
      pin(ob[m]);
    }
  };
  static_assert(UB0 >= 3 && UB1 >= 3 && UB1 <= 7 && DA <= 4, "ring barrier");
  static_for<0, 10>([&](auto UU) {
    constexpr int U = UU;
    mm(UU);
    if constexpr (U + 1 < 4) rdC(IC<(U + 1 < 4 ? U + 1 : 0)>{});
    if constexpr (U + DA < 4) lda(IC<(U + DA < 4 ? U + DA : 0)>{});
    if constexpr (U == WL_U) static_for<2 * NL, 2 * NW>(fillW);
    if constexpr (U == 4) static_for<NL, NW>(rdW);
    if constexpr (U == U_L) ld1_sc1<Z + ID_P3>(poll, rsWS, fvo, mbr);
    if constexpr (U == U_L) {
      resid();
      need<Z + ID_P3>(poll);
      rdy = (unsigned)__builtin_amdgcn_ballot_w64(poll == tag);
      static_for<4, U_L + 1 + DA>(lda);
    }
    if constexpr (U > U_L && U + DA <= 9) lda(IC<(U + DA <= 9 ? U + DA : 0)>{});
    if constexpr (U == (SSM ? UB0 : UB1)) bar();
  });
  {
    unsigned a = L_RED + ((wv * 64u + r) * 68u + 4u * h) * 4u;
    pin(a);
#pragma unroll
    for (int g = 0; g < 4; ++g) {
      *(LDSP f32x4*)(a + 32u * g) = f32x4{c00[4 * g], c00[4 * g + 1], c00[4 * g + 2], c00[4 * g + 3]};
      *(LDSP f32x4*)(a + 32u * g + 8704u) = f32x4{c01[4 * g], c01[4 * g + 1], c01[4 * g + 2], c01[4 * g + 3]};
      *(LDSP f32x4*)(a + 32u * g + 128u) = f32x4{c10[4 * g], c10[4 * g + 1], c10[4 * g + 2], c10[4 * g + 3]};
      *(LDSP f32x4*)(a + 32u * g + 8832u) = f32x4{c11[4 * g], c11[4 * g + 1], c11[4 * g + 2], c11[4 * g + 3]};
    }
  }
  unsigned ra0 = L_RED + ((tid >> 4) * 68u + (tid & 15u) * 4u) * 4u, ra1 = ra0 + 3u * 17408u, ra2 = ra0 + 6u * 17408u;
  float* op = p.out + (size_t)(rt2 * 128u + orow) * DM + ocol;
  pin(ra0); pin(ra1); pin(ra2); pin(op);
  fence();
  bar();
  if constexpr (!SSM) st1_if(rg | (rt2 ^ 15u) | ct | rh, tag, rsWS, lane == 0u ? 0u : OOB, WS_EPO);
#pragma unroll
  for (unsigned m = 0; m < 2; ++m) {
    const unsigned o = m * 8704u;
    const f32x4 t0 = (lds128f(ra0 + o) + lds128f(ra0 + o + 17408u)) + (lds128f(ra0 + o + 34816u) + lds128f(ra1 + o));
    const f32x4 t1 = (lds128f(ra1 + o + 17408u) + lds128f(ra1 + o + 34816u)) + (lds128f(ra2 + o) + lds128f(ra2 + o + 17408u));
    const f32x4 v = (t0 + t1) + ob[m];
    asm volatile("global_store_dwordx4 %0, %1, off sc1\n\ts_nop 1" :: "v"(op + m * (32u * DM)), "v"(v) : "memory");
  }
  asm volatile("\n.Ls5end%c[z]:" :: [z] "i"(Z) : "memory");
  }
}

#if defined(__HIP_DEVICE_COMPILE__)
__attribute__((target("no-packed-fp32-ops")))
#endif
__global__ __launch_bounds__(512, 2) void s5_geglu_kernel(const Args p) {
  __shared__ __attribute__((aligned(16))) char smem[LDS_BYTES];
  asm volatile("" :: "v"((unsigned)(uintptr_t)(LDSP char*)smem));
  const unsigned b = blockIdx.x, xcd = b & 7u, idx = b >> 3;
  const unsigned rt = (xcd >> 1) * 4u + (idx & 3u), s = (xcd & 1u) * 8u + (idx >> 2);
  const unsigned wv = __builtin_amdgcn_readfirstlane(threadIdx.x >> 6);
#if defined(__HIP_DEVICE_COMPILE__)
  {
    unsigned long long pc;
    unsigned span;
    asm volatile("s_getpc_b64 %0\n.Ls5beg:\n\ts_mov_b32 %1, .Ls5end0-.Ls5beg\n\ts_max_u32 %1, %1, .Ls5end1000-.Ls5beg" : "=s"(pc), "=s"(span) :: "scc");
    const unsigned ln = threadIdx.x & 63u;
    touch_if(wv ^ 3u, mk_rsrc((const void*)(pc & ~127ull), span), ln < 8u ? ln * 4096u : OOB, idx * 128u);
  }
  if (wv < 4u) body<true>(p, rt, s, wv);
  else body<false>(p, rt, s, wv & 3u);
#endif
}

extern "C" void kernel_launch(void* const* d_in, const int* in_sizes, int n_in, void* d_out, int out_size, void* d_ws,
                              size_t ws_size, hipStream_t stream) {
  Args a;
  a.x = (const u16*)d_in[0]; a.lre = (const u16*)d_in[1]; a.lim = (const u16*)d_in[2]; a.Bre = (const u16*)d_in[3];
  a.Bim = (const u16*)d_in[4]; a.Cre = (const u16*)d_in[5]; a.Cim = (const u16*)d_in[6]; a.ldt = (const u16*)d_in[7];
  a.Dsk = (const u16*)d_in[8]; a.wg = (const u16*)d_in[10]; a.wu = (const u16*)d_in[11]; a.wd = (const u16*)d_in[12];
  a.out = (float*)d_out; a.ws = (char*)d_ws;
  s5_geglu_kernel<<<256, 512, 0, stream>>>(a);
}
```
